```python
import torch

def _softmax(x, axis=-1):
    return torch.softmax(x, dim=axis)
def _gelu(x):
    return torch.nn.functional.gelu(x, approximate="tanh")
def _silu(x):
    return torch.nn.functional.silu(x)
def _sigmoid(x):
    return torch.sigmoid(x)
def _layernorm(x, axis=-1, eps=1e-5):
    m = x.mean(dim=axis, keepdim=True)
    v = x.var(dim=axis, keepdim=True, unbiased=False)
    return (x - m) / torch.sqrt(v + eps)
def _scatter_add(tgt, idx, src, axis=0):
    return tgt.clone().index_add_(axis, idx, src)
def _segment_sum(src, seg, n):
    out = torch.zeros((n,) + src.shape[1:], dtype=src.dtype)
    return out.index_add_(0, seg, src)
def _pad(x, pw):
    flat = tuple(v for pair in reversed(pw) for v in pair)
    return torch.nn.functional.pad(x, flat)

kernel_name = 'gated_mix__softmax_full_mha__ssm_conv_s4__prompt'

def setup_inputs():

    _w = (lambda a: (lambda t: t.to(torch.bfloat16) if t.is_floating_point() else t)(torch.as_tensor(a)))
    def _A_setup():
        B, S, D = 1, 2048, 512
        return {
            "x": _w((lambda *s: torch.randn(*s, dtype=torch.bfloat16) * 0.05)(B, S, D)),
            "wqkv": _w((lambda *s: torch.randn(*s, dtype=torch.bfloat16) * 0.8)(D, 3 * D)),
            "wo": _w((lambda *s: torch.randn(*s, dtype=torch.bfloat16) * 0.05)(D, D)),
        }
    _A_in = _A_setup()

    def _B_setup():
        B, L, D, N = 1, 2048, 512, 128
        A_re = -(lambda _x: torch.exp(torch.as_tensor(_x)))((lambda *s: torch.randn(*s, dtype=torch.bfloat16) * 6.4)(D, N))
        A_im = torch.pi * (lambda *s: torch.randn(*s, dtype=torch.bfloat16) * 0.1)(D, N)
        B_re = (lambda *s: torch.randn(*s, dtype=torch.bfloat16) * 0.2)(D, N)
        B_im = (lambda *s: torch.randn(*s, dtype=torch.bfloat16) * 0.2)(D, N)
        C_re = (lambda *s: torch.randn(*s, dtype=torch.bfloat16) * 0.2)(D, N)
        C_im = (lambda *s: torch.randn(*s, dtype=torch.bfloat16) * 0.2)(D, N)
        log_dt = (lambda *s: torch.randn(*s, dtype=torch.bfloat16) * 0.1)(D) * 0.5 - 4.0
        Dskip = (lambda *s: torch.randn(*s, dtype=torch.bfloat16) * 0.1)(D)
        return {
            "u": _w((lambda *s: torch.randn(*s, dtype=torch.bfloat16) * 0.05)(B, L, D)),
            "A_re": _w(A_re),
            "A_im": _w(A_im),
            "B_re": _w(B_re),
            "B_im": _w(B_im),
            "C_re": _w(C_re),
            "C_im": _w(C_im),
            "log_dt": _w(log_dt),
            "Dskip": _w(Dskip),
        }
    _B_in = _B_setup()

    return {
        "A_x": _A_in["x"],
        "A_wqkv": _A_in["wqkv"],
        "A_wo": _A_in["wo"],
        "B_u": _B_in["u"],
        "B_A_re": _B_in["A_re"],
        "B_A_im": _B_in["A_im"],
        "B_B_re": _B_in["B_re"],
        "B_B_im": _B_in["B_im"],
        "B_C_re": _B_in["C_re"],
        "B_C_im": _B_in["C_im"],
        "B_log_dt": _B_in["log_dt"],
        "B_Dskip": _B_in["Dskip"],
        "gate_w": _w((lambda *s: torch.randn(*s, dtype=torch.bfloat16) * 0.05)(512) * 16.0),
    }

def reference(A_x, A_wqkv, A_wo, B_u, B_A_re, B_A_im, B_B_re, B_B_im, B_C_re, B_C_im, B_log_dt, B_Dskip, gate_w):
    def _A_ref(x, wqkv, wo):
        B, S, D = x.shape
        H = 32; Dh = D // H
        qkv = (lambda x, *d: x.permute(*d))((x @ wqkv).reshape(B, S, 3, H, Dh), 2, 0, 3, 1, 4)
        q, k, v = qkv[0], qkv[1], qkv[2]
        scale = Dh ** -0.5
        scores = torch.einsum("bhqd,bhkd->bhqk", q, k) * scale
        p = _softmax(scores, axis=-1)
        o = torch.einsum("bhqk,bhkd->bhqd", p, v)
        return (lambda x, *d: x.permute(*d))(o, 0, 2, 1, 3).reshape(B, S, D) @ wo

    def _B_ref(u, A_re, A_im, B_re, B_im, C_re, C_im, log_dt, Dskip):
        Bsz, L, D = u.shape
        dt = torch.exp(log_dt)[:, None]
        A = A_re + 1j * A_im
        Bc = B_re + 1j * B_im
        Cc = C_re + 1j * C_im
        logdA = dt * A
        dB = dt * Bc
        l = (lambda x, dt: x.to(dt))(torch.arange(L), A_re.dtype)
        P = torch.exp(logdA[:, :, None] * l[None, None, :])
        K = torch.einsum("dn,dnl,dn->dl", Cc, P, dB).real
        L2 = 2 * L
        Kp = _pad(K, ((0, 0), (0, L)))
        up = (lambda x, *d: x.permute(*d))(u, 0, 2, 1)
        up = _pad(up, ((0, 0), (0, 0), (0, L)))
        Kf = (lambda x, axes=(-2,-1): torch.fft.rfft2(x.float(), dim=axes))(Kp[:, None, :], axes=(-2, -1))
        Uf = (lambda x, axes=(-2,-1): torch.fft.rfft2(x.float(), dim=axes))(up[:, :, None, :], axes=(-2, -1))
        Yf = Uf * Kf[None, :, :, :]
        y = (lambda x, s, axes=(-2,-1): torch.fft.irfft2(x, s=s, dim=axes).to(torch.bfloat16))(Yf, (1, L2), axes=(-2, -1))[:, :, 0, :L]
        y = (lambda x, *d: x.permute(*d))(y, 0, 2, 1) + u * Dskip[None, None, :]
        return y

    x = A_x
    g = _sigmoid((x * gate_w).sum(-1, keepdims=True))
    a = _A_ref(x, A_wqkv, A_wo)
    b = _B_ref(x, B_A_re, B_A_im, B_B_re, B_B_im, B_C_re, B_C_im, B_log_dt, B_Dskip)
    return g * a + (1 - g) * b
```

```cpp
#include <hip/hip_runtime.h>
#include <hip/hip_bf16.h>
#include <stdint.h>

typedef float f32x4 __attribute__((ext_vector_type(4)));
typedef float f32x2 __attribute__((ext_vector_type(2)));
typedef float f32x8 __attribute__((ext_vector_type(8)));
typedef float f32x16 __attribute__((ext_vector_type(16)));
typedef __bf16 bf16x8 __attribute__((ext_vector_type(8)));
typedef __bf16 bf16x16 __attribute__((ext_vector_type(16)));
typedef __bf16 bf16x4 __attribute__((ext_vector_type(4)));
typedef __bf16 bf16x2 __attribute__((ext_vector_type(2)));
typedef short s16x4 __attribute__((ext_vector_type(4)));
typedef unsigned u32x4 __attribute__((ext_vector_type(4)));
typedef unsigned u32x2 __attribute__((ext_vector_type(2)));
typedef unsigned short u16;
#define LAS __attribute__((address_space(3)))

constexpr int S = 2048, D = 512, H = 32, NM = 128;
constexpr int VCH = 528;
constexpr int KIMG = 65536, VIMG = 128 * VCH;
constexpr int KVIMG = KIMG + VIMG;
constexpr size_t WS_Q = 0;
constexpr size_t WS_KV = WS_Q + (size_t)H * 65536;
constexpr size_t WS_BT = WS_KV + (size_t)H * KVIMG;
constexpr size_t WS_O = WS_BT + (size_t)D * S * 2;
constexpr float QSCALE = 0.25f * 1.4426950408889634f;

__device__ __forceinline__ float bf2f(u16 v) { return __uint_as_float((unsigned)v << 16); }
__device__ __forceinline__ unsigned pk2(float lo, float hi) { bf16x2 v = {(__bf16)lo, (__bf16)hi}; return __builtin_bit_cast(unsigned, v); }
__device__ __forceinline__ float rbf(float v) { return bf2f(__builtin_bit_cast(u16, (__bf16)v)); }
__device__ __forceinline__ s16x4 trread(LAS const char* p) { return __builtin_amdgcn_ds_read_tr16_b64_v4i16((LAS s16x4*)p); }
__device__ __forceinline__ bf16x8 cat8(s16x4 a, s16x4 b) {
    typedef short s16x8 __attribute__((ext_vector_type(8)));
    s16x8 r = {a[0], a[1], a[2], a[3], b[0], b[1], b[2], b[3]}; return __builtin_bit_cast(bf16x8, r); }

__global__ void __launch_bounds__(512) k_qkv(const u16* __restrict__ x, const u16* __restrict__ w, char* __restrict__ ws) {
    extern __shared__ __attribute__((aligned(16))) char smem[];
    const int tid = threadIdx.x, lane = tid & 63, wid = tid >> 6;
    const int bid = blockIdx.x, h = (bid & 7) * 4 + ((bid >> 3) & 3), qt = bid >> 5;
    for (int i = tid; i < 3 * 512 * 2; i += 512) {
        const int m = i >> 10, k = (i >> 1) & 511, half = i & 1;
        const u32x4 v = *(const u32x4*)(w + (size_t)k * 1536 + m * 512 + h * 16 + half * 8);
        *(u32x4*)(smem + m * 16384 + k * 32 + half * 16) = v;
    }
    char* xs = smem + 49152 + wid * 8192;
    const int row0 = qt * 256 + wid * 32;
    f32x4 acc[2][3];
    for (int a = 0; a < 2; ++a) for (int m = 0; m < 3; ++m) acc[a][m] = f32x4{0, 0, 0, 0};
    __syncthreads();
    const int g = lane >> 4, r16 = lane & 15;
    LAS const char* wbase = (LAS const char*)smem + (8 * g + (r16 >> 2)) * 32 + (lane & 3) * 8;
    for (int kc = 0; kc < 4; ++kc) {
        u32x4 st[8];
#pragma unroll
        for (int i = 0; i < 8; ++i) { const int row = i * 4 + (lane >> 4), ch = lane & 15;
            st[i] = *(const u32x4*)(x + (size_t)(row0 + row) * 512 + kc * 128 + ch * 8); }
#pragma unroll
        for (int i = 0; i < 8; ++i) { const int row = i * 4 + (lane >> 4), ch = lane & 15;
            *(u32x4*)(xs + row * 256 + ((ch ^ (row & 15)) * 16)) = st[i]; }
        asm volatile("" ::: "memory");
#pragma unroll
        for (int ks = 0; ks < 4; ++ks) {
            bf16x8 xf[2], wf[3];
#pragma unroll
            for (int a = 0; a < 2; ++a) xf[a] = *(const bf16x8*)(xs + (a * 16 + r16) * 256 + (((ks * 4 + g) ^ r16) * 16));
#pragma unroll
            for (int m = 0; m < 3; ++m) { LAS const char* p = wbase + m * 16384 + (kc * 128 + ks * 32) * 32;
                wf[m] = cat8(trread(p), trread(p + 128)); }
#pragma unroll
            for (int a = 0; a < 2; ++a) {
                acc[a][0] = __builtin_amdgcn_mfma_f32_16x16x32_bf16(wf[0], xf[a], acc[a][0], 0, 0, 0);
                acc[a][1] = __builtin_amdgcn_mfma_f32_16x16x32_bf16(wf[1], xf[a], acc[a][1], 0, 0, 0);
                acc[a][2] = __builtin_amdgcn_mfma_f32_16x16x32_bf16(xf[a], wf[2], acc[a][2], 0, 0, 0);
            }
        }
        asm volatile("" ::: "memory");
    }
    const int tile = qt * 8 + wid;
    char* qimg = ws + WS_Q + (size_t)h * 65536 + tile * 1024;
    char* kimg = ws + WS_KV + (size_t)h * KVIMG + tile * 1024;
    char* vimg = ws + WS_KV + (size_t)h * KVIMG + KIMG + (size_t)tile * 2 * VCH;
#pragma unroll
    for (int a = 0; a < 2; ++a) {
        const int off = (g >> 1) * 512 + (a * 16 + r16) * 16 + (g & 1) * 8;
        *(u32x2*)(qimg + off) = u32x2{pk2(acc[a][0][0] * QSCALE, acc[a][0][1] * QSCALE), pk2(acc[a][0][2] * QSCALE, acc[a][0][3] * QSCALE)};
        *(u32x2*)(kimg + off) = u32x2{pk2(acc[a][1][0], acc[a][1][1]), pk2(acc[a][1][2], acc[a][1][3])};
        *(u32x2*)(vimg + a * VCH + (g >> 1) * 256 + r16 * 16 + (g & 1) * 8) = u32x2{pk2(acc[a][2][0], acc[a][2][1]), pk2(acc[a][2][2], acc[a][2][3])};
    }
    if (lane < 2) *(u32x4*)(vimg + lane * VCH + 512) = u32x4{0x3f803f80u, 0x3f803f80u, 0x3f803f80u, 0x3f803f80u};
}

__global__ void __launch_bounds__(512) k_attn(char* __restrict__ ws) {
    extern __shared__ __attribute__((aligned(16))) char smem[];
    const int tid = threadIdx.x, lane = tid & 63, wid = tid >> 6;
    const int bid = blockIdx.x, h = (bid & 7) * 4 + ((bid >> 3) & 3), qt = bid >> 5;
    const char* kv = ws + WS_KV + (size_t)h * KVIMG;
    for (int i = tid; i < KVIMG / 16; i += 512) *(u32x4*)(smem + i * 16) = *(const u32x4*)(kv + i * 16);
    const int tile = qt * 8 + wid;
    const bf16x8 qf = *(const bf16x8*)(ws + WS_Q + (size_t)h * 65536 + tile * 1024 + lane * 16);
    __syncthreads();
    const int r = lane & 31, hh = lane >> 5;
    const char* kp = smem + lane * 16;
    const char* vp = smem + KIMG + (r < 16 ? hh * 256 + r * 16 : 512);
    f32x16 zero16; for (int i = 0; i < 16; ++i) zero16[i] = 0.f;
    f32x16 negm;
    {
        f32x16 s = __builtin_amdgcn_mfma_f32_32x32x16_bf16(*(const bf16x8*)kp, qf, zero16, 0, 0, 0);
        float m = s[0];
        for (int i = 1; i < 16; ++i) m = fmaxf(m, s[i]);
        m = fmaxf(m, __shfl_xor(m, 32));
        for (int i = 0; i < 16; ++i) negm[i] = -m;
    }
    f32x16 o = zero16;
#pragma unroll 4
    for (int t = 0; t < 64; ++t) {
        const bf16x8 kf = *(const bf16x8*)(kp + t * 1024);
        const bf16x8 v0 = *(const bf16x8*)(vp + t * 2 * VCH);
        const bf16x8 v1 = *(const bf16x8*)(vp + t * 2 * VCH + VCH);
        f32x16 s = __builtin_amdgcn_mfma_f32_32x32x16_bf16(kf, qf, negm, 0, 0, 0);
        f32x8 p0, p1;
#pragma unroll
        for (int i = 0; i < 8; ++i) { p0[i] = __builtin_amdgcn_exp2f(s[i]); p1[i] = __builtin_amdgcn_exp2f(s[8 + i]); }
        o = __builtin_amdgcn_smfmac_f32_32x32x32_bf16(v0, __builtin_bit_cast(bf16x16, p0), o, (int)0xDDDDDDDD, 0, 0);
        o = __builtin_amdgcn_smfmac_f32_32x32x32_bf16(v1, __builtin_bit_cast(bf16x16, p1), o, (int)0xDDDDDDDD, 0, 0);
    }
    float l = o[8];
    if (!__all(l > 0x1p-30f && l < 0x1p60f)) {
        float m = -INFINITY; o = zero16;
        for (int t = 0; t < 64; ++t) {
            const bf16x8 kf = *(const bf16x8*)(kp + t * 1024);
            const bf16x8 v0 = *(const bf16x8*)(vp + t * 2 * VCH);
            const bf16x8 v1 = *(const bf16x8*)(vp + t * 2 * VCH + VCH);
            f32x16 s = __builtin_amdgcn_mfma_f32_32x32x16_bf16(kf, qf, zero16, 0, 0, 0);
            float tm = s[0];
            for (int i = 1; i < 16; ++i) tm = fmaxf(tm, s[i]);
            tm = fmaxf(tm, __shfl_xor(tm, 32));
            const float mn = fmaxf(m, tm);
            const float al = __builtin_amdgcn_exp2f(m - mn);
            m = mn;
            f32x8 p0, p1;
            for (int i = 0; i < 8; ++i) { p0[i] = __builtin_amdgcn_exp2f(s[i] - m); p1[i] = __builtin_amdgcn_exp2f(s[8 + i] - m); }
            for (int i = 0; i < 16; ++i) o[i] *= al;
            o = __builtin_amdgcn_smfmac_f32_32x32x32_bf16(v0, __builtin_bit_cast(bf16x16, p0), o, (int)0xDDDDDDDD, 0, 0);
            o = __builtin_amdgcn_smfmac_f32_32x32x32_bf16(v1, __builtin_bit_cast(bf16x16, p1), o, (int)0xDDDDDDDD, 0, 0);
        }
        l = o[8];
    }
    const float rl = 1.0f / l;
    u16* O = (u16*)(ws + WS_O) + (size_t)(tile * 32 + r) * 512 + h * 16 + hh * 4;
    *(u32x2*)(O) = u32x2{pk2(o[0] * rl, o[1] * rl), pk2(o[2] * rl, o[3] * rl)};
    *(u32x2*)(O + 8) = u32x2{pk2(o[4] * rl, o[5] * rl), pk2(o[6] * rl, o[7] * rl)};
}

__device__ __forceinline__ f32x2 cmul(f32x2 a, f32x2 b) { return f32x2{a[0] * b[0] - a[1] * b[1], a[0] * b[1] + a[1] * b[0]}; }
__device__ __forceinline__ f32x2 cexpm(float are_l2, float aim_rev, float mult) {
    const float e = __builtin_amdgcn_exp2f(are_l2 * mult);
    const float f = __builtin_amdgcn_fractf(aim_rev * mult);
    return f32x2{e * __builtin_amdgcn_cosf(f), e * __builtin_amdgcn_sinf(f)};
}
constexpr int TROW = 528;
constexpr int KCP = 4224;
__global__ void __launch_bounds__(512) k_ssm(const u16* __restrict__ x, const u16* __restrict__ Are, const u16* __restrict__ Aim,
                                             const u16* __restrict__ Bre, const u16* __restrict__ Bim, const u16* __restrict__ Cre,
                                             const u16* __restrict__ Cim, const u16* __restrict__ logdt, const u16* __restrict__ dskip,
                                             char* __restrict__ ws) {
    extern __shared__ __attribute__((aligned(16))) char smem[];
    const int tid = threadIdx.x, lane = tid & 63, wid = tid >> 6;
    const int c = wid >> 2, sub = wid & 3, d = blockIdx.x * 2 + c;
    char* tabX = smem + c * (64 * TROW);
    char* tabY = tabX + 32 * TROW;
    float* kt = (float*)(smem + 2 * 64 * TROW) + c * 1024;
    char* kc = smem + 2 * 64 * TROW + 8192 + c * (4 * KCP);
    char* ub = smem + 2 * 64 * TROW + 8192 + 8 * KCP + c * 7680;
    for (int t = tid; t < S; t += 512) {
        const unsigned v = *(const unsigned*)(x + (size_t)t * 512 + blockIdx.x * 2);
        char* u0 = smem + 2 * 64 * TROW + 8192 + 8 * KCP;
        *(u16*)(u0 + (32 + (t >> 5)) * 80 + (t & 31) * 2) = (u16)(v & 0xffff);
        *(u16*)(u0 + 7680 + (32 + (t >> 5)) * 80 + (t & 31) * 2) = (u16)(v >> 16);
    }
    for (int i = tid; i < 2 * 160; i += 512) { char* u0 = smem + 2 * 64 * TROW + 8192 + 8 * KCP; *(u32x4*)(u0 + (i / 160) * 7680 + (i % 160) * 16) = u32x4{0, 0, 0, 0}; }
    {
        const int n = (sub & 1) * 64 + lane;
        const float dt = rbf(__expf(bf2f(logdt[d])));
        const float are = dt * bf2f(Are[d * NM + n]), aim = dt * bf2f(Aim[d * NM + n]);
        const float al2 = are * 1.4426950408889634f, rev = aim * 0.15915494309189535f;
        if (sub < 2) {
            for (int j = 0; j < 32; ++j) { const f32x2 e = cexpm(al2, rev, 8.f * j); *(unsigned*)(tabY + j * TROW + n * 4) = pk2(e[0], e[1]); }
        } else {
            const f32x2 db = {dt * bf2f(Bre[d * NM + n]), dt * bf2f(Bim[d * NM + n])};
            const f32x2 wv = cmul(f32x2{bf2f(Cre[d * NM + n]), bf2f(Cim[d * NM + n])}, db);
            for (int i = 0; i < 32; ++i) {
                const int hi = i < 8 ? i : i < 12 ? 256 + 2 * (i - 8) : i < 16 ? 512 + 256 * ((i - 12) >> 1) + 4 * (i & 1) : i < 21 ? 1024 + 256 * (i - 16) : 0;
                const f32x2 e = cmul(wv, cexpm(al2, rev, (float)hi));
                *(unsigned*)(tabX + i * TROW + n * 4) = pk2(e[0], -e[1]);
            }
        }
    }
    __syncthreads();
    if (sub == 0) {
        f32x16 acc; for (int i = 0; i < 16; ++i) acc[i] = 0.f;
        const int r = lane & 31, hh = lane >> 5;
        for (int s = 0; s < 16; ++s) {
            const bf16x8 a = *(const bf16x8*)(tabX + r * TROW + s * 32 + hh * 16);
            const bf16x8 b = *(const bf16x8*)(tabY + r * TROW + s * 32 + hh * 16);
            acc = __builtin_amdgcn_mfma_f32_32x32x16_bf16(a, b, acc, 0, 0, 0);
        }
        for (int i = 0; i < 16; ++i) kt[((i & 3) + 8 * (i >> 2) + 4 * hh) * 32 + r] = acc[i];
    }
    __syncthreads();
    for (int i = (tid & 255); i < 2112; i += 256) {
        for (int k = 0; k < 4; ++k) {
            const int l = 2048 + k - i;
            float v = 0.f;
            if (l >= 0 && l < 2048) {
                const int lb = (int)rbf((float)l);
                int row, col;
                if (lb < 256) { row = lb & 7; col = lb >> 3; }
                else if (lb < 512) { row = 8 + ((lb >> 1) & 3); col = (lb - 256) >> 3; }
                else if (lb < 1024) { row = 12 + 2 * ((lb - 512) >> 8) + ((lb >> 2) & 1); col = (lb & 255) >> 3; }
                else { row = 16 + ((lb - 1024) >> 8); col = (lb & 255) >> 3; }
                v = kt[row * 32 + col];
            }
            *(u16*)(kc + k * KCP + i * 2) = __builtin_bit_cast(u16, (__bf16)v);
        }
    }
    __syncthreads();
    {
        const int r = lane & 31, hh = lane >> 5;
        f32x16 y0, y1; for (int i = 0; i < 16; ++i) { y0[i] = 0.f; y1[i] = 0.f; }
        const char* ap = kc + (r & 3) * KCP + (2048 - (r & ~3) + 8 * hh) * 2;
        const char* bp = ub + (32 + r) * 80 + hh * 16;
        for (int dl = sub; dl < 32; dl += 4) {
#pragma unroll
            for (int kk = 0; kk < 2; ++kk) {
                const char* a0 = ap + (16 * kk - 32 * dl) * 2;
                const char* a1 = a0 - 2048;
                const u32x2 x0 = *(const u32x2*)a0, x1 = *(const u32x2*)(a0 + 8), z0 = *(const u32x2*)a1, z1 = *(const u32x2*)(a1 + 8);
                const bf16x8 A0 = __builtin_bit_cast(bf16x8, u32x4{x0[0], x0[1], x1[0], x1[1]});
                const bf16x8 A1 = __builtin_bit_cast(bf16x8, u32x4{z0[0], z0[1], z1[0], z1[1]});
                const bf16x8 B0 = *(const bf16x8*)(bp - dl * 80 + kk * 32);
                const bf16x8 B1 = *(const bf16x8*)(bp + (32 - dl) * 80 + kk * 32);
                y0 = __builtin_amdgcn_mfma_f32_32x32x16_bf16(A0, B0, y0, 0, 0, 0);
                y1 = __builtin_amdgcn_mfma_f32_32x32x16_bf16(A0, B1, y1, 0, 0, 0);
                y1 = __builtin_amdgcn_mfma_f32_32x32x16_bf16(A1, B0, y1, 0, 0, 0);
            }
        }
        float* red = (float*)(smem + c * (64 * TROW)) + sub * 2048;
        for (int i = 0; i < 16; ++i) { const int b = (i & 3) + 8 * (i >> 2) + 4 * hh; red[r * 32 + b] = y0[i]; red[1024 + r * 32 + b] = y1[i]; }
    }
    __syncthreads();
    {
        const float* red = (const float*)(smem + c * (64 * TROW));
        const float ds = bf2f(dskip[d]);
        u16* bt = (u16*)(ws + WS_BT) + (size_t)d * S;
        const int t0 = (tid & 255) * 8;
        float v[8];
        for (int i = 0; i < 8; ++i) { const int t = t0 + i;
            const float y = red[t] + red[2048 + t] + red[4096 + t] + red[6144 + t];
            const float u = bf2f(*(const u16*)(ub + (32 + (t >> 5)) * 80 + (t & 31) * 2));
            v[i] = rbf(y) + rbf(u * ds); }
        *(u32x4*)(bt + t0) = u32x4{pk2(v[0], v[1]), pk2(v[2], v[3]), pk2(v[4], v[5]), pk2(v[6], v[7])};
    }
}

__global__ void __launch_bounds__(256) k_out(const u16* __restrict__ x, const u16* __restrict__ wo, const u16* __restrict__ gw,
                                             const char* __restrict__ ws, u16* __restrict__ out) {
    extern __shared__ __attribute__((aligned(16))) char smem[];
    const int tid = threadIdx.x, lane = tid & 63, wid = tid >> 6;
    const int rt = blockIdx.x >> 3, ct = blockIdx.x & 7;
    for (int i = tid; i < 512 * 8; i += 256) { const int k = i >> 3, ch = i & 7;
        *(u32x4*)(smem + (ch >> 1) * 16384 + k * 32 + (ch & 1) * 16) = *(const u32x4*)(wo + (size_t)k * 512 + ct * 64 + ch * 8); }
    const int g = lane >> 4, r16 = lane & 15;
    const int row = rt * 64 + wid * 16 + r16;
    const u16* O = (const u16*)(ws + WS_O) + (size_t)row * 512 + g * 8;
    bf16x8 of[16];
    float gs = 0.f;
#pragma unroll
    for (int ks = 0; ks < 16; ++ks) of[ks] = *(const bf16x8*)(O + ks * 32);
#pragma unroll
    for (int ks = 0; ks < 16; ++ks) {
        const u32x4 xv = *(const u32x4*)(x + (size_t)row * 512 + ks * 32 + g * 8), gv = *(const u32x4*)(gw + ks * 32 + g * 8);
        for (int j = 0; j < 4; ++j) { gs += rbf(__uint_as_float(xv[j] << 16) * __uint_as_float(gv[j] << 16)); gs += rbf(__uint_as_float(xv[j] & 0xffff0000u) * __uint_as_float(gv[j] & 0xffff0000u)); }
    }
    gs += __shfl_xor(gs, 16); gs += __shfl_xor(gs, 32);
    const float gate = rbf(1.0f / (1.0f + __expf(-rbf(gs)))), omg = rbf(1.0f - gate);
    __syncthreads();
    f32x4 acc[4]; for (int m = 0; m < 4; ++m) acc[m] = f32x4{0, 0, 0, 0};
    LAS const char* wbase = (LAS const char*)smem + (8 * g + (r16 >> 2)) * 32 + (lane & 3) * 8;
#pragma unroll
    for (int ks = 0; ks < 16; ++ks)
#pragma unroll
        for (int m = 0; m < 4; ++m) { LAS const char* p = wbase + m * 16384 + ks * 1024;
            acc[m] = __builtin_amdgcn_mfma_f32_16x16x32_bf16(cat8(trread(p), trread(p + 128)), of[ks], acc[m], 0, 0, 0); }
    const u16* bt = (const u16*)(ws + WS_BT);
#pragma unroll
    for (int m = 0; m < 4; ++m) { float v[4];
        for (int i = 0; i < 4; ++i) { const int n = ct * 64 + 16 * m + 4 * g + i;
            v[i] = rbf(gate * rbf(acc[m][i])) + rbf(omg * bf2f(bt[(size_t)n * S + row])); }
        *(u32x2*)(out + (size_t)row * 512 + ct * 64 + 16 * m + 4 * g) = u32x2{pk2(v[0], v[1]), pk2(v[2], v[3])};
    }
}

extern "C" void kernel_launch(void* const* d_in, const int* in_sizes, int n_in, void* d_out, int out_size, void* d_ws, size_t ws_size, hipStream_t stream) {
    const u16* x = (const u16*)d_in[0];
    char* ws = (char*)d_ws;
    constexpr int LDS_QKV = 49152 + 8 * 8192, LDS_ATT = KVIMG, LDS_SSM = 2 * 64 * TROW + 8192 + 8 * KCP + 2 * 7680, LDS_OUT = 65536;
    (void)hipFuncSetAttribute((const void*)k_qkv, hipFuncAttributeMaxDynamicSharedMemorySize, LDS_QKV);
    (void)hipFuncSetAttribute((const void*)k_attn, hipFuncAttributeMaxDynamicSharedMemorySize, LDS_ATT);
    (void)hipFuncSetAttribute((const void*)k_ssm, hipFuncAttributeMaxDynamicSharedMemorySize, LDS_SSM);
    (void)hipFuncSetAttribute((const void*)k_out, hipFuncAttributeMaxDynamicSharedMemorySize, LDS_OUT);
    k_qkv<<<256, 512, LDS_QKV, stream>>>(x, (const u16*)d_in[1], ws);
    k_ssm<<<256, 512, LDS_SSM, stream>>>(x, (const u16*)d_in[4], (const u16*)d_in[5], (const u16*)d_in[6], (const u16*)d_in[7], (const u16*)d_in[8],
                                         (const u16*)d_in[9], (const u16*)d_in[10], (const u16*)d_in[11], ws);
    k_attn<<<256, 512, LDS_ATT, stream>>>(ws);
    k_out<<<256, 256, LDS_OUT, stream>>>(x, (const u16*)d_in[2], (const u16*)d_in[12], ws, (u16*)d_out);
}
```

```cpp
#include <hip/hip_runtime.h>
#include <hip/hip_bf16.h>
#include <stdint.h>

typedef float f32x2 __attribute__((ext_vector_type(2)));
typedef float f32x4 __attribute__((ext_vector_type(4)));
typedef float f32x8 __attribute__((ext_vector_type(8)));
typedef float f32x16 __attribute__((ext_vector_type(16)));
typedef __bf16 bf16x2 __attribute__((ext_vector_type(2)));
typedef __bf16 bf16x8 __attribute__((ext_vector_type(8)));
typedef __bf16 bf16x16 __attribute__((ext_vector_type(16)));
typedef short s16x4 __attribute__((ext_vector_type(4)));
typedef unsigned u32x2 __attribute__((ext_vector_type(2)));
typedef unsigned u32x4 __attribute__((ext_vector_type(4)));
typedef unsigned short u16;
typedef unsigned long long u64;
#define LAS __attribute__((address_space(3)))
#define GAS __attribute__((address_space(1)))
#define CAS __attribute__((address_space(4)))
#define RLX_AGENT __ATOMIC_RELAXED, __HIP_MEMORY_SCOPE_AGENT
#define SC1 16
#define NA 2
#define LTRIP 4
#define ELOW 7
#define ROT rt
#define CPRIO 3

constexpr unsigned WS_NONCE = 0, WS_F1 = 4096, WS_F2 = WS_F1 + 65536, WS_F3 = WS_F2 + 65536;
constexpr unsigned WS_G = WS_F3 + 65536;
constexpr unsigned WS_KV = 262144, KVT = 2304;
constexpr unsigned WS_XT = WS_KV + 32 * 64 * KVT;
constexpr unsigned WS_BT = WS_XT + 2097152;
constexpr unsigned WS_OF = WS_BT + 2097152;
constexpr unsigned WS_END = WS_OF + 2097152;
constexpr unsigned WS_STAMP = 16u << 20;
constexpr unsigned SLOT = 24576, TROW = 528;
constexpr unsigned slot_at(int c) { return ((c + 4) % 6) * SLOT; }
constexpr unsigned L_LO = 73728, LOC = 32 * TROW;
constexpr unsigned L_HI = 107520, HIC = 27 * TROW;
constexpr unsigned L_PAR = 147456;
constexpr unsigned L_CTL = 163712, LDS_BYTES = 163840;
constexpr unsigned L_QX = 65536;
constexpr unsigned KCP = 4224;
constexpr unsigned L_UB = 147456, UBC = 7680;
constexpr unsigned L_PA = 107520;
constexpr unsigned L_XB = 73728, XBW = 2560;
constexpr unsigned L_BI = 107520, BROW = 144, L_RED = 122880;
enum { C_KV = 0, C_WQ = 4, C_U = 8, C_CONV = 12, C_TAB = 16, C_R7 = 20, C_GO = 24, C_E = 32 };
constexpr float QSCALE = 0.25f * 1.4426950408889634f;
constexpr int SMF_IDX = (int)0xDDDDDDDD;

__device__ __forceinline__ float bf2f(unsigned v) { return __uint_as_float(v << 16); }
__device__ __forceinline__ float bfhi(unsigned v) { return __uint_as_float(v & 0xffff0000u); }
__device__ __forceinline__ unsigned pk2(float lo, float hi) { bf16x2 v = {(__bf16)lo, (__bf16)hi}; return __builtin_bit_cast(unsigned, v); }
__device__ __forceinline__ float rbf(float v) { return bf2f(__builtin_bit_cast(u16, (__bf16)v)); }
__device__ __forceinline__ u32x2 trread(unsigned a) { return __builtin_bit_cast(u32x2, __builtin_amdgcn_ds_read_tr16_b64_v4i16((LAS s16x4*)a)); }
__device__ __forceinline__ bf16x8 trfrag(unsigned a0, unsigned a1) { const u32x2 a = trread(a0), b = trread(a1); return __builtin_bit_cast(bf16x8, u32x4{a[0], a[1], b[0], b[1]}); }
template <typename T> __device__ __forceinline__ T ldsr(unsigned a) { return *(LAS const T*)a; }
template <typename T> __device__ __forceinline__ void ldsw(unsigned a, T v) { *(LAS T*)a = v; }
__device__ __forceinline__ void glds_s(const void* sbase, unsigned voff, unsigned lds) {
    asm volatile("s_mov_b32 m0, %2\n\ts_nop 0\n\tglobal_load_lds_dwordx4 %0, %1" :: "v"(voff), "s"(sbase), "s"(lds) : "memory");
}
__device__ __forceinline__ void glds_v(const void* vaddr, unsigned lds) {
    asm volatile("s_mov_b32 m0, %1\n\ts_nop 0\n\tglobal_load_lds_dwordx4 %0, off" :: "v"(vaddr), "s"(lds) : "memory");
}
#define BAR() asm volatile("s_waitcnt lgkmcnt(0)\n\ts_barrier" ::: "memory")
#define VBAR(n) asm volatile("s_waitcnt vmcnt(" #n ") lgkmcnt(0)\n\ts_barrier" ::: "memory")
#define SBAR() __builtin_amdgcn_sched_barrier(0)
#define MFMA32(a, b, c) __builtin_amdgcn_mfma_f32_32x32x16_bf16(a, b, c, 0, 0, 0)
#define MFMA16(a, b, c) __builtin_amdgcn_mfma_f32_16x16x32_bf16(a, b, c, 0, 0, 0)
#define SMF(a, p, o) __builtin_amdgcn_smfmac_f32_32x32x32_bf16(a, __builtin_bit_cast(bf16x16, p), o, SMF_IDX, 0, 0)
#define EXP2(v) __builtin_amdgcn_exp2f(v)
#define STAMP(i)
#define STAMP_DECL
#define STAMP_OUT()

struct C2 { float r0, i0, r1, i1; };
__device__ __forceinline__ C2 cmul2(const C2& a, const C2& b) {
    C2 d;
    asm("v_mul_f32 %0, %4, %8\n\tv_mul_f32 %1, %5, %8\n\tv_mul_f32 %2, %6, %10\n\tv_mul_f32 %3, %7, %10\n\t"
        "v_fma_f32 %0, -%5, %9, %0\n\tv_fma_f32 %1, %4, %9, %1\n\tv_fma_f32 %2, -%7, %11, %2\n\tv_fma_f32 %3, %6, %11, %3"
        : "=&v"(d.r0), "=&v"(d.i0), "=&v"(d.r1), "=&v"(d.i1) : "v"(a.r0), "v"(a.i0), "v"(a.r1), "v"(a.i1), "v"(b.r0), "v"(b.i0), "v"(b.r1), "v"(b.i1));
    return d;
}
__device__ __forceinline__ u32x2 cpack(const C2& a) { return u32x2{pk2(a.r0, a.i0), pk2(a.r1, a.i1)}; }
__device__ __forceinline__ f32x2 cexpm(float al2, float rev, float mult) {
    const float e = EXP2(al2 * mult), f = __builtin_amdgcn_fractf(rev * mult);
    return f32x2{e * __builtin_amdgcn_cosf(f), e * __builtin_amdgcn_sinf(f)};
}
__device__ __forceinline__ C2 cexp2(float l0, float v0, float l1, float v1, float mult) {
    const f32x2 a = cexpm(l0, v0, mult), b = cexpm(l1, v1, mult); return C2{a[0], a[1], b[0], b[1]};
}
__device__ __forceinline__ void wait_flags(GAS u64* p, u64 tag) {
    for (int spins = 0; spins < (1 << 18); ++spins) {
        if (__all(__hip_atomic_load(p, RLX_AGENT) == tag)) break;
        __builtin_amdgcn_s_sleep(2);
    }
}
__device__ __forceinline__ f32x8 lo8(const f32x16& s) { return __builtin_shufflevector(s, s, 0, 1, 2, 3, 4, 5, 6, 7); }
__device__ __forceinline__ f32x8 hi8(const f32x16& s) { return __builtin_shufflevector(s, s, 8, 9, 10, 11, 12, 13, 14, 15); }
__device__ __forceinline__ float rowmax(const f32x16& s) {
    float m = fmaxf(fmaxf(s[0], s[1]), s[2]);
#pragma unroll
    for (int i = 3; i < 15; i += 2) m = fmaxf(fmaxf(m, s[i]), s[i + 1]);
    m = fmaxf(m, s[15]);
    const auto r = __builtin_amdgcn_permlane32_swap(__float_as_uint(m), __float_as_uint(m), false, false);
    return fmaxf(__uint_as_float(r[0]), __uint_as_float(r[1]));
}

struct Params {
    const u16 *x, *wqkv, *wo, *are, *aim, *bre, *bim, *cre, *cim, *logdt, *dskip, *gw;
    u16* out; char* ws;
};
__device__ __forceinline__ unsigned lds_add(unsigned a) { return __hip_atomic_fetch_add((LAS unsigned*)a, 1u, __ATOMIC_RELAXED, __HIP_MEMORY_SCOPE_WORKGROUP); }
__device__ __forceinline__ void lds_wait(unsigned a, unsigned n) {
    for (int sp = 0; sp < (1 << 22) && *(volatile LAS unsigned*)a < n; ++sp) __builtin_amdgcn_s_sleep(1);
    asm volatile("" ::: "memory");
}
__global__ void __launch_bounds__(512, 2) fused_kernel(Params P) {
    char* const ws = P.ws;
    const int tid = threadIdx.x, lane = tid & 63, wid = __builtin_amdgcn_readfirstlane(tid >> 6), bid = blockIdx.x;
    const int xcd = bid & 7, jb = bid >> 3;
    const int hg = (xcd % (8 / NA)) * NA + (jb % NA), rt = (xcd / (8 / NA)) * (32 / NA) + jb / NA;
    const int ort = 4 * xcd + (jb >> 3), oc = jb & 7;
    const int c0 = 64 * hg + 2 * rt;
    const int r = lane & 31, hh = lane >> 5, g = lane >> 4, r16 = lane & 15;
    STAMP_DECL; STAMP(0);
    asm volatile("" :: "s"(P.x), "s"(P.wqkv), "s"(P.are), "s"(P.aim), "s"(P.bre), "s"(P.bim), "s"(P.cre), "s"(P.cim), "s"(P.logdt), "s"(P.dskip), "s"(P.gw), "s"(P.ws), "s"(P.wo), "s"(P.out));
    const int tt = wid & 1, pp = (wid >> 1) & 1;
    f32x16 zero16;
#pragma unroll
    for (int i = 0; i < 16; ++i) zero16[i] = 0.f;
    const __amdgpu_buffer_rsrc_t rs = __builtin_amdgcn_make_buffer_rsrc(ws, 0, WS_END, 0x00020000);
    u64 tag;
    unsigned dk2;

    if (__builtin_expect(wid < 4, 1)) {
        const unsigned vx0 = (16 * wid + (lane >> 3)) * 1024 + (((lane & 7) ^ g) << 4), vx1 = vx0 + 8192 + ((((lane & 7) ^ g ^ 4) - ((lane & 7) ^ g)) << 4);
        const unsigned vw = (16 * wid + (lane >> 3)) * 3072 + (((((lane & 7) >> 1) ^ ((g & 1) << 1)) * 2 + (lane & 1)) << 4);
        const unsigned vw1 = vw + 24576;
        const char* xb = (const char*)P.x + rt * 65536;
        const char* wb = (const char*)P.wqkv + hg * 128;
        auto dmaW = [&](int m, int cc, unsigned dst) { const char* s = wb + m * 1024 + ((cc + hg) & 7) * 196608;
            glds_s(s, vw, dst + wid * 2048); glds_s(s, vw1, dst + wid * 2048 + 1024); };
        auto dmaC = [&](int cc) { const unsigned sl = slot_at(cc); const char* s = xb + ((cc + hg) & 7) * 128;
            glds_s(s, vx0, sl + wid * 2048); glds_s(s, vx1, sl + wid * 2048 + 1024); dmaW(1, cc, sl + 8192); dmaW(2, cc, sl + 16384); };
        dmaC(0);
        {
            const int a = wid < 3 ? wid : 2;
            const u16* s0 = a == 0 ? P.are : a == 1 ? P.bre : P.cre;
            const u16* s1 = a == 0 ? P.aim : a == 1 ? P.bim : P.cim;
            glds_v((const char*)(hh ? s1 : s0) + c0 * 256 + r * 16, L_PAR + a * 1024);
        }
        STAMP(13);
        dmaC(1); dmaC(2);
        STAMP(14);
        const unsigned ld2 = ((CAS const unsigned*)P.logdt)[c0 >> 1];
        dk2 = ((CAS const unsigned*)P.dskip)[c0 >> 1];
        const float dt = rbf(__expf(tt ? bfhi(ld2) : bf2f(ld2 & 0xffff)));
        u32x2 lv[16], hv[14];
#define PINV(v, n) _Pragma("unroll") for (int i_ = 0; i_ < (n); ++i_) asm volatile("" : "+v"(v[i_]))
        VBAR(12); STAMP(1); dmaC(3);
        const unsigned pb = L_PAR + tt * 256 + lane * 4;
        const unsigned pr = ldsr<unsigned>(pb), pi = ldsr<unsigned>(pb + 512), br = ldsr<unsigned>(pb + 1024), bi = ldsr<unsigned>(pb + 1536), cr = ldsr<unsigned>(pb + 2048), ci = ldsr<unsigned>(pb + 2560);
        VBAR(12); STAMP(16); dmaC(4);
        const float l0 = dt * bf2f(pr & 0xffff) * 1.4426950408889634f, l1 = dt * bfhi(pr) * 1.4426950408889634f;
        const float v0 = dt * bf2f(pi & 0xffff) * 0.15915494309189535f, v1 = dt * bfhi(pi) * 0.15915494309189535f;
        C2 e = cexp2(l0, v0, l1, v1, 8.f), a = cexp2(l0, v0, l1, v1, 128.f * pp);
#define LO(j0, j1) _Pragma("unroll") for (int j = (j0); j < (j1); ++j) { lv[j] = cpack(a); if (j < 15) a = cmul2(a, e); } PINV(lv, 16); SBAR()
        SBAR();
        VBAR(12); STAMP(17); dmaC(5); LO(0, 4);
        VBAR(12); STAMP(18); dmaC(6); LO(4, 8);
        VBAR(12); STAMP(19); dmaC(7); LO(8, 12);
        VBAR(12); STAMP(20); dmaW(0, 0, 0); dmaW(0, 1, 8192); dmaW(0, 2, 16384); LO(12, 16);
        VBAR(12); STAMP(21); dmaW(0, 3, 24576); dmaW(0, 4, 32768); dmaW(0, 5, 40960);
        a = cmul2(C2{bf2f(cr & 0xffff), -bf2f(ci & 0xffff), bfhi(cr), -bfhi(ci)}, C2{dt * bf2f(br & 0xffff), -dt * bf2f(bi & 0xffff), dt * bfhi(br), -dt * bfhi(bi)});
        const C2 gg = cexp2(l0, -v0, l1, -v1, 256.f);
        e = cexp2(l0, -v0, l1, -v1, pp ? 4.f : 1.f);
        SBAR();
        VBAR(12); STAMP(22); dmaW(0, 6, 49152); dmaW(0, 7, 57344);
        tag = __hip_atomic_load((GAS u64*)(ws + WS_NONCE), RLX_AGENT) + 1;
        hv[13] = u32x2{0, 0};
        if (pp == 0) {
#pragma unroll
            for (int j = 0; j < 9; ++j) { if (j < 8) hv[j] = cpack(a); if (!(j & 1)) hv[8 + (j >> 1)] = cpack(cmul2(a, gg)); if (j < 8) a = cmul2(a, e); }
            PINV(hv, 13);
        } else {
            const C2 f = cmul2(e, e);
#pragma unroll
            for (int k = 1; k < 8; ++k) { a = cmul2(a, gg); if (k >= 2) { const int row = k < 4 ? 3 * (k - 2) : 6 + 2 * (k - 4); hv[row] = cpack(a);
                if (k < 4) hv[row + 1] = cpack(cmul2(a, e)); hv[row + (k < 4 ? 2 : 1)] = cpack(cmul2(a, f)); } }
            PINV(hv, 14);
        }
        STAMP(2);
        asm volatile("s_waitcnt vmcnt(0)" ::: "memory");
        if (lane == 0) lds_add(L_CTL + C_WQ);
        lds_wait(L_CTL + C_R7, 4);
        STAMP(3);
        const unsigned tl = L_LO + tt * LOC + 16 * pp * TROW + lane * 8, th = L_HI + tt * HIC + 13 * pp * TROW + lane * 8;
#pragma unroll
        for (int i = 0; i < 16; ++i) { ldsw<u32x2>(tl + i * TROW, lv[i]); if (i < 13 || (i == 13 && pp)) ldsw<u32x2>(th + i * TROW, hv[i]); }
        asm volatile("s_waitcnt lgkmcnt(0)" ::: "memory");
        if (lane == 0) lds_add(L_CTL + C_TAB);
        STAMP(23);
        if (wid == 3) {
            wait_flags((GAS u64*)(ws + WS_F1 + (hg * 32 + r) * 256), tag);
            if (lane == 0) lds_add(L_CTL + C_GO);
        } else if (wid == 2) {
            lds_wait(L_CTL + C_TAB, 4);
#pragma unroll
            for (int i = 0; i < 5; ++i) ldsw<u32x4>(L_UB + hh * UBC + (r + 32 * i) * 16, u32x4{0, 0, 0, 0});
        } else {
            const int c = wid;
            const u64 mp = r < 8 ? 0x0706050403020100ull : r < 16 ? 0x121110100b0a0908ull : r < 24 ? 0x000000120f0e0d0cull : 0x1a19181716151413ull;
            const unsigned aa = L_HI + c * HIC + (unsigned)((mp >> ((r & 7) * 8)) & 0xff) * TROW + hh * 16, ba = L_LO + c * LOC + r * TROW + hh * 16;
            lds_wait(L_CTL + C_TAB, 4);
            bf16x8 ta[16], tb[16];
#pragma unroll
            for (int s = 0; s < 4; ++s) { ta[s] = ldsr<bf16x8>(aa + s * 32); tb[s] = ldsr<bf16x8>(ba + s * 32); }
            SBAR();
            f32x16 k = zero16;
#pragma unroll
            for (int s = 0; s < 16; ++s) { k = MFMA32(ta[s], tb[s], k); SBAR(); if (s < 12) { ta[s + 4] = ldsr<bf16x8>(aa + s * 32 + 128); tb[s + 4] = ldsr<bf16x8>(ba + s * 32 + 128); } SBAR(); }
            STAMP(15);
            asm volatile("" ::: "memory");
            const unsigned K0 = L_LO + c * LOC, R = K0 + (2040 - 8 * r) * 2;
            ldsw<u32x2>(R + (1 - hh) * 8, u32x2{pk2(k[3], k[2]), pk2(k[1], k[0])});
            ldsw<u32x4>(R - (32 + 64 * hh) * 16, u32x4{pk2(k[8], k[7]), pk2(k[6], k[6]), pk2(k[6], k[5]), pk2(k[4], k[4])});
            if (hh == 0) ldsw<u32x4>(R - 64 * 16, u32x4{pk2(k[11], k[11]), pk2(k[10], k[10]), pk2(k[10], k[9]), pk2(k[9], k[9])});
#pragma unroll
            for (int e_ = 0; e_ < 2; ++e_) { const float a_ = k[12 + 2 * e_], b_ = k[13 + 2 * e_]; const unsigned aa2 = pk2(a_, a_);
                ldsw<u32x4>(R - (128 + 32 * e_ + 64 * hh) * 16, u32x4{pk2(b_, b_), pk2(b_, (r & 1) ? b_ : a_), aa2, aa2}); }
            if (lane < 8) ldsw<u32x4>(K0 + 4096 + lane * 16, u32x4{0, 0, 0, 0});
            asm volatile("" ::: "memory");
            u32x4 cu[5]; u32x2 pv[5];
#pragma unroll
            for (int ps = 0; ps < 5; ++ps) { const unsigned a_ = K0 + (ps < 4 ? 64 * ps + lane : 256 + (lane & 7)) * 16; cu[ps] = ldsr<u32x4>(a_); pv[ps] = ldsr<u32x2>(a_ - 8); }
#pragma unroll
            for (int ps = 0; ps < 5; ++ps) {
                const unsigned am = __builtin_amdgcn_alignbit(pv[ps][1], pv[ps][0], 16), a0 = __builtin_amdgcn_alignbit(cu[ps][0], pv[ps][1], 16), a1 = __builtin_amdgcn_alignbit(cu[ps][1], cu[ps][0], 16),
                               a2 = __builtin_amdgcn_alignbit(cu[ps][2], cu[ps][1], 16), a3 = __builtin_amdgcn_alignbit(cu[ps][3], cu[ps][2], 16);
                const unsigned W = K0 + KCP + (ps < 4 ? 64 * ps + lane : 256 + (lane & 7)) * 16;
                ldsw<u32x4>(W, u32x4{a0, a1, a2, a3}); ldsw<u32x4>(W + KCP, u32x4{pv[ps][1], cu[ps][0], cu[ps][1], cu[ps][2]}); ldsw<u32x4>(W + 2 * KCP, u32x4{am, a0, a1, a2});
            }
        }
    } else {
        if (wid == 4 && lane < 16) ldsw<unsigned>(L_CTL + lane * 4, 0u);
        __builtin_amdgcn_s_setprio(2);
        const int w4 = wid - 4;
        tag = __hip_atomic_load((GAS u64*)(ws + WS_NONCE), RLX_AGENT) + 1;
        {
            const int grow = rt * 64 + 8 * hg + 2 * w4;
            const u32x4 gv = *(const u32x4*)(P.gw + lane * 8), x0 = *(const u32x4*)(P.x + grow * 512 + lane * 8), x1 = *(const u32x4*)(P.x + grow * 512 + 512 + lane * 8);
            float s0 = 0.f, s1 = 0.f;
#pragma unroll
            for (int j = 0; j < 4; ++j) { const unsigned gj = gv[j], aj = x0[j], bj = x1[j];
                s0 = __builtin_amdgcn_fdot2_f32_bf16(__builtin_bit_cast(bf16x2, aj), __builtin_bit_cast(bf16x2, gj), s0, false);
                s1 = __builtin_amdgcn_fdot2_f32_bf16(__builtin_bit_cast(bf16x2, bj), __builtin_bit_cast(bf16x2, gj), s1, false);
            }
            const auto sa = __builtin_amdgcn_permlane32_swap(__float_as_uint(s0), __float_as_uint(s1), false, false);
            const unsigned t0 = __float_as_uint(__uint_as_float(sa[0]) + __uint_as_float(sa[1]));
            const auto sc = __builtin_amdgcn_permlane16_swap(t0, t0, false, false);
            float t = __uint_as_float(sc[0]) + __uint_as_float(sc[1]);
            t += __uint_as_float(__builtin_amdgcn_update_dpp(0u, __float_as_uint(t), 0x128, 0xf, 0xf, false));
            t += __uint_as_float(__builtin_amdgcn_update_dpp(0u, __float_as_uint(t), 0x124, 0xf, 0xf, false));
            t += __uint_as_float(__builtin_amdgcn_update_dpp(0u, __float_as_uint(t), 0x122, 0xf, 0xf, false));
            t += __uint_as_float(__builtin_amdgcn_update_dpp(0u, __float_as_uint(t), 0x121, 0xf, 0xf, false));
            const float gate = rbf(1.0f / (1.0f + __expf(-rbf(t))));
            if (r == 0) __builtin_amdgcn_raw_buffer_store_b32(__float_as_uint(gate), rs, WS_G + (grow + hh) * 4, 0, SC1);
        }
        dk2 = ((CAS const unsigned*)P.dskip)[c0 >> 1];
        unsigned xa[2][4], wk[2];
#pragma unroll
        for (int ks = 0; ks < 4; ++ks) { xa[0][ks] = (32 * tt + r) * 128 + (((2 * ks + hh) ^ ((r >> 1) & 7)) << 4); xa[1][ks] = xa[0][ks] + 3 * SLOT; asm volatile("" : "+v"(xa[1][ks])); }
        const unsigned wa = (8 * hh + (r16 >> 2)) * 128 + (((2 * pp + (g & 1)) ^ (r16 >> 3 << 1)) << 5) + (lane & 3) * 8;
        wk[0] = wa + 8192; wk[1] = wa + 8192 + 3 * SLOT; asm volatile("" : "+v"(wk[0]), "+v"(wk[1]));
        f32x16 ak = zero16, av = zero16;
        bf16x8 xq[32];
        u32x2 fk[4][2], fv[4][2];
#define SH(cc) (slot_at(cc) >= 3 * SLOT)
#define SO(cc) (slot_at(cc) % (3 * SLOT))
#define RX(cc, ks) xq[4 * (cc) + (ks)] = ldsr<bf16x8>(xa[SH(cc)][ks] + SO(cc))
#define RK(cc, ks, t) fk[ks][t] = trread(wk[SH(cc)] + SO(cc) + (ks) * 2048 + (t) * 512)
#define RV(cc, ks, t) fv[ks][t] = trread(wk[SH(cc)] + SO(cc) + 8192 + (ks) * 2048 + (t) * 512)
#define FR(f, ks) __builtin_bit_cast(bf16x8, u32x4{f[ks][0][0], f[ks][0][1], f[ks][1][0], f[ks][1][1]})
#define MK(cc, ks) ak = MFMA32(FR(fk, ks), xq[4 * (cc) + (ks)], ak); SBAR()
#define MV(cc, ks) av = MFMA32(xq[4 * (cc) + (ks)], FR(fv, ks), av); SBAR()
#pragma unroll
        for (int cc = 0; cc < 8; ++cc) {
            STAMP(14 + cc); BAR();
            if (cc) { MK(cc - 1, 2); } RX(cc, 0); RK(cc, 0, 0); RK(cc, 0, 1); SBAR();
            if (cc) { MV(cc - 1, 2); } RV(cc, 0, 0); RV(cc, 0, 1); RX(cc, 1); RK(cc, 1, 0); SBAR();
            if (cc) { MK(cc - 1, 3); } RK(cc, 1, 1); RV(cc, 1, 0); RV(cc, 1, 1); SBAR();
            if (cc) { MV(cc - 1, 3); } RX(cc, 2); RK(cc, 2, 0); RK(cc, 2, 1); RV(cc, 2, 0); SBAR();
            MK(cc, 0); RV(cc, 2, 1); RX(cc, 3); RK(cc, 3, 0); SBAR();
            MV(cc, 0); RK(cc, 3, 1); RV(cc, 3, 0); RV(cc, 3, 1); SBAR();
            if (cc == 0) {
                u32x2 t[4];
#pragma unroll
                for (int n = 0; n < 4; ++n) { const int row = 16 * g + 4 * n + (r16 >> 2);
                    t[n] = trread(slot_at(0) + row * 128 + (((2 * w4 + ((lane & 3) >> 1)) ^ ((row >> 1) & 7)) << 4) + (lane & 1) * 8); }
                MK(cc, 1); MV(cc, 1);
                const unsigned o = WS_XT + (64 * hg + 16 * w4 + r16) * 4096 + rt * 128 + g * 32;
                __builtin_amdgcn_raw_buffer_store_b128(u32x4{t[0][0], t[0][1], t[1][0], t[1][1]}, rs, o, 0, SC1);
                __builtin_amdgcn_raw_buffer_store_b128(u32x4{t[2][0], t[2][1], t[3][0], t[3][1]}, rs, o + 16, 0, SC1);
                SBAR();
            } else { MK(cc, 1); MV(cc, 1); }
        }
        MK(7, 2); MV(7, 2); MK(7, 3); MV(7, 3);
        STAMP(1);
        {
            const unsigned tb = WS_KV + ((4 * hg + 2 * pp) * 64 + 2 * rt + tt) * KVT;
            const int rp = (r & 19) | ((r & 4) << 1) | ((r & 8) >> 1);
            const unsigned vb = tb + (r >> 4) * (64 * KVT) + 1024;
#pragma unroll
            for (int e = 0; e < 2; ++e) {
                __builtin_amdgcn_raw_buffer_store_b128(u32x4{pk2(ak[8 * e], ak[8 * e + 1]), pk2(ak[8 * e + 2], ak[8 * e + 3]), pk2(ak[8 * e + 4], ak[8 * e + 5]), pk2(ak[8 * e + 6], ak[8 * e + 7])},
                                                       rs, tb + e * (64 * KVT) + hh * 512 + rp * 16, 0, SC1);
                __builtin_amdgcn_raw_buffer_store_b128(u32x4{pk2(av[8 * e], av[8 * e + 1]), pk2(av[8 * e + 2], av[8 * e + 3]), pk2(av[8 * e + 4], av[8 * e + 5]), pk2(av[8 * e + 6], av[8 * e + 7])},
                                                       rs, vb + e * 640 + hh * 256 + r16 * 16, 0, SC1);
            }
            if (r16 == 0) __builtin_amdgcn_raw_buffer_store_b128(u32x4{0x3f803f80u, 0x3f803f80u, 0x3f803f80u, 0x3f803f80u}, rs, vb + hh * 640 + 512, 0, SC1);
        }
        if (lane == 0) lds_add(L_CTL + C_R7);
        STAMP(2);
        asm volatile("s_waitcnt vmcnt(0) lgkmcnt(0)" ::: "memory");
        if (lane == 0 && lds_add(L_CTL + C_KV) == 3u) __hip_atomic_store((GAS u64*)(ws + WS_F1 + (hg * 32 + rt) * 256), tag, RLX_AGENT);
        STAMP(3);
        lds_wait(L_CTL + C_WQ, 4);
        STAMP(23);
        f32x16 aq = zero16;
        bf16x8 wq[8];
#pragma unroll
        for (int ks = 0; ks < 8; ++ks) wq[ks] = trfrag(ks * 2048 + wa, ks * 2048 + wa + 512);
        SBAR();
#pragma unroll
        for (int i = 0; i < 32; ++i) {
            aq = MFMA32(wq[i & 7], xq[i], aq); SBAR();
            if (i < 24) wq[i & 7] = trfrag((i + 8) * 2048 + wa, (i + 8) * 2048 + wa + 512);
            SBAR();
        }
#pragma unroll
        for (int e_ = 0; e_ < 2; ++e_)
            ldsw<u32x4>(L_QX + ((2 * pp + e_) * 2 + tt) * 1024 + lane * 16,
                        u32x4{pk2(aq[8 * e_] * QSCALE, aq[8 * e_ + 1] * QSCALE), pk2(aq[8 * e_ + 2] * QSCALE, aq[8 * e_ + 3] * QSCALE),
                              pk2(aq[8 * e_ + 4] * QSCALE, aq[8 * e_ + 5] * QSCALE), pk2(aq[8 * e_ + 6] * QSCALE, aq[8 * e_ + 7] * QSCALE)});
        asm volatile("s_waitcnt lgkmcnt(0)" ::: "memory");
        if (lane == 0) lds_add(L_CTL + C_GO);
    }
    STAMP(24);
    lds_wait(L_CTL + C_GO, 5);
    STAMP(4);
    const int hd = wid & 3, kh = wid >> 2;
    if (kh) __builtin_amdgcn_s_setprio(2); else __builtin_amdgcn_s_setprio(3);
    const unsigned voffK = lane * 16, voffV = r < 16 ? 1024 + hh * 256 + r * 16 : (r == 16 || r == 20) ? 1536 : 0x4000000;
    const unsigned kvh = WS_KV + (4 * hg + hd) * 64 * KVT;
    unsigned vK[4], vV[4];
#pragma unroll
    for (int u = 0; u < 4; ++u) { vK[u] = voffK + u * KVT; vV[u] = voffV + u * KVT; asm volatile("" : "+v"(vK[u]), "+v"(vV[u])); }
    auto tsoff = [&](int tr) -> unsigned { return tr < 8 ? kvh + (32 * kh + 4 * ((tr + ROT) & 7)) * KVT : WS_END; };
    bf16x8 kf[4], va[4], vb[4];
#define LOADT(u, so_) do { \
        va[u] = __builtin_bit_cast(bf16x8, __builtin_amdgcn_raw_buffer_load_b128(rs, vV[u], so_, SC1)); \
        vb[u] = __builtin_bit_cast(bf16x8, __builtin_amdgcn_raw_buffer_load_b128(rs, vV[u] + 640, so_, SC1)); \
        kf[u] = __builtin_bit_cast(bf16x8, __builtin_amdgcn_raw_buffer_load_b128(rs, vK[u], so_, SC1)); } while (0)
    unsigned soc = tsoff(0);
    u32x4 uv[2];
    if (!kh) {
#pragma unroll
        for (int c = 0; c < 2; ++c) uv[c] = __builtin_amdgcn_raw_buffer_load_b128(rs, WS_XT + (c0 + c) * 4096 + tid * 16, 0, SC1);
    }
    LOADT(0, soc); LOADT(1, soc); LOADT(2, soc);
    SBAR();
    vb[3] = __builtin_bit_cast(bf16x8, u32x4{0, 0, 0, 0});
    const bf16x8 qa = ldsr<bf16x8>(L_QX + hd * 2048 + lane * 16), qb = ldsr<bf16x8>(L_QX + hd * 2048 + 1024 + lane * 16);
    if (!kh) {
#pragma unroll
        for (int c = 0; c < 2; ++c) ldsw<u32x4>(L_UB + c * UBC + (32 + (tid >> 2)) * 80 + (tid & 3) * 16, uv[c]);
        asm volatile("s_waitcnt lgkmcnt(0)" ::: "memory");
        if (lane == 0) lds_add(L_CTL + C_U);
    }
    f32x16 nma, nmb, oa = zero16, ob = zero16, sa, sb = zero16;
    { const float ma = -rowmax(MFMA32(kf[0], qa, zero16)), mb = -rowmax(MFMA32(kf[0], qb, zero16));
#pragma unroll
      for (int i = 0; i < 16; ++i) { nma[i] = ma; nmb[i] = mb; } }
    sa = MFMA32(kf[0], qa, nma);
    STAMP(5);
#define EX(s, a, b) _Pragma("unroll") for (int i_ = (a); i_ < (b); ++i_) s[i_] = EXP2(s[i_]); SBAR()
    auto attn = [&](int t0, int t1) {
        for (int tr = t0; tr < t1; ++tr) {
            const unsigned son = tsoff(tr + 1);
            if (tr == ELOW && !kh) {
                __builtin_amdgcn_s_setprio(0);
                if (wid == 0) wait_flags((GAS u64*)(ws + WS_F2 + (oc * 32 + r) * 256), tag);
            }
#pragma unroll
            for (int u = 0; u < 4; ++u) {
                EX(sa, 0, 2); ob = SMF(vb[(u + 3) & 3], hi8(sb), ob); SBAR(); LOADT((u + 3) & 3, u ? son : soc); SBAR();
                EX(sa, 2, 5); sb = MFMA32(kf[u], qb, nmb); SBAR();
                EX(sa, 5, 11); oa = SMF(va[u], lo8(sa), oa); SBAR();
                EX(sa, 11, 16);
                EX(sb, 0, 2); oa = SMF(vb[u], hi8(sa), oa); SBAR();
                EX(sb, 2, 5); sa = MFMA32(kf[(u + 1) & 3], qa, nma); SBAR();
                EX(sb, 5, 11); ob = SMF(va[u], lo8(sb), ob); SBAR();
                EX(sb, 11, 16);
            }
            soc = son;
        }
    };
    const int ctrip = kh ? LTRIP : 0;
    attn(0, ctrip);
    STAMP(6);
    {
        __builtin_amdgcn_s_setprio(CPRIO);
        const int c = wid & 1, e = (wid >> 1) & 1, dlt = 16 * kh + 8 * e + 7;
        const int kap = (r + 1) & 3;
        unsigned ap = L_LO + c * LOC + kap * KCP + (2047 - r + kap + 8 * hh - 32 * dlt) * 2 - 2048;
        unsigned bp = L_UB + c * UBC + (32 + r - dlt) * 80 + hh * 16;
        const unsigned pA = L_PA + (c * 2 + e) * 8192 + lane * 16;
        f32x16 y0 = zero16, y1 = zero16;
        lds_wait(L_CTL + C_U, 4);
        if (kh) {
            lds_wait(L_CTL + C_E + 4 * (c * 2 + e), 1);
#pragma unroll
            for (int i = 0; i < 4; ++i) { const f32x4 p = ldsr<f32x4>(pA + i * 1024), q = ldsr<f32x4>(pA + 4096 + i * 1024);
#pragma unroll
                for (int k = 0; k < 4; ++k) { y0[4 * i + k] = p[k]; y1[4 * i + k] = q[k]; } }
        }
        struct Fr { u32x2 a0, a1, z0, z1; bf16x8 b0, b1; } f[2];
#define CA(i) (ap + 32 * (i))
#define CB(i) (bp + 80 * ((i) >> 1) + 32 * ((i) & 1))
#define CAT(x, y) __builtin_bit_cast(bf16x8, u32x4{x[0], x[1], y[0], y[1]})
        f[0].a0 = ldsr<u32x2>(CA(0) + 2048); f[0].a1 = ldsr<u32x2>(CA(0) + 2056); f[0].b0 = ldsr<bf16x8>(CB(0));
        f[0].b1 = ldsr<bf16x8>(CB(0) + 2560); f[0].z0 = ldsr<u32x2>(CA(0)); f[0].z1 = ldsr<u32x2>(CA(0) + 8);
        SBAR();
#pragma unroll
        for (int i = 0; i < 16; ++i) {
            Fr& q = f[i & 1]; Fr& n = f[(i + 1) & 1];
            y0 = MFMA32(CAT(q.a0, q.a1), q.b0, y0); SBAR();
            if (i < 15) { n.a0 = ldsr<u32x2>(CA(i + 1) + 2048); n.a1 = ldsr<u32x2>(CA(i + 1) + 2056); n.b0 = ldsr<bf16x8>(CB(i + 1)); } SBAR();
            y1 = MFMA32(CAT(q.a0, q.a1), q.b1, y1); SBAR();
            if (i < 15) { n.b1 = ldsr<bf16x8>(CB(i + 1) + 2560); n.z0 = ldsr<u32x2>(CA(i + 1)); n.z1 = ldsr<u32x2>(CA(i + 1) + 8); } SBAR();
            y1 = MFMA32(CAT(q.z0, q.z1), q.b0, y1); SBAR();
        }
        STAMP(27);
#pragma unroll
        for (int i = 0; i < 4; ++i) {
            if (!kh || e) ldsw<f32x4>(pA + i * 1024, f32x4{y0[4 * i], y0[4 * i + 1], y0[4 * i + 2], y0[4 * i + 3]});
            if (!kh || !e) ldsw<f32x4>(pA + 4096 + i * 1024, f32x4{y1[4 * i], y1[4 * i + 1], y1[4 * i + 2], y1[4 * i + 3]});
        }
        asm volatile("s_waitcnt lgkmcnt(0)" ::: "memory");
        if (lane == 0) lds_add(L_CTL + C_E + 4 * (c * 2 + e));
        if (kh) {
            lds_wait(L_CTL + C_E + 4 * (c * 2 + 1 - e), 2);
            f32x16 y = e ? y1 : y0;
            const unsigned pO = L_PA + (c * 2 + 1 - e) * 8192 + e * 4096 + lane * 16;
#pragma unroll
            for (int i = 0; i < 4; ++i) { const f32x4 p = ldsr<f32x4>(pO + i * 1024);
#pragma unroll
                for (int k = 0; k < 4; ++k) y[4 * i + k] += p[k]; }
            const float dsk = c ? bfhi(dk2) : bf2f(dk2 & 0xffff);
            u32x2 w[4];
#pragma unroll
            for (int i = 0; i < 4; ++i) {
                const u32x2 u = ldsr<u32x2>(L_UB + c * UBC + (32 + 32 * e + r) * 80 + i * 16 + hh * 8);
                const unsigned q0 = pk2(y[4 * i], y[4 * i + 1]), q1 = pk2(y[4 * i + 2], y[4 * i + 3]);
                const unsigned s0 = pk2(bf2f(u[0] & 0xffff) * dsk, bfhi(u[0]) * dsk), s1 = pk2(bf2f(u[1] & 0xffff) * dsk, bfhi(u[1]) * dsk);
                w[i] = u32x2{pk2(bf2f(q0 & 0xffff) + bf2f(s0 & 0xffff), bfhi(q0) + bfhi(s0)), pk2(bf2f(q1 & 0xffff) + bf2f(s1 & 0xffff), bfhi(q1) + bfhi(s1))};
            }
#pragma unroll
            for (int i = 0; i < 4; i += 2) {
                const auto s0 = __builtin_amdgcn_permlane32_swap(w[i][0], w[i + 1][0], false, false), s1 = __builtin_amdgcn_permlane32_swap(w[i][1], w[i + 1][1], false, false);
                __builtin_amdgcn_raw_buffer_store_b128(u32x4{s0[0], s1[0], s0[1], s1[1]}, rs, WS_BT + (c0 + c) * 4096 + (32 * e + r) * 64 + i * 16 + hh * 16, 0, SC1);
            }
            asm volatile("s_waitcnt vmcnt(0) lgkmcnt(0)" ::: "memory");
            if (lane == 0 && lds_add(L_CTL + C_CONV) == 3u) __hip_atomic_store((GAS u64*)(ws + WS_F2 + (hg * 32 + rt) * 256), tag, RLX_AGENT);
        } else {
            const unsigned vo = (128 * wid + (lane >> 3)) * 1024 + (((((lane & 7) >> 1) ^ g) * 2 + (lane & 1)) << 4);
            const char* s = (const char*)P.wo + oc * 128;
#pragma unroll
            for (int i = 0; i < 16; ++i) glds_s(s + i * 8192, vo, wid * 16384 + i * 1024);
        }
        __builtin_amdgcn_s_setprio(1);
    }
    STAMP(7);
    attn(ctrip, 8);
    ob = SMF(vb[3], hi8(sb), ob);
    STAMP(8);
    {
        const unsigned xw = L_XB + wid * XBW, xr = L_XB + (wid ^ 4) * XBW;
        const f32x16 os = kh ? oa : ob; f32x16 o = kh ? ob : oa;
        const float ms = kh ? nma[0] : nmb[0], mk = kh ? nmb[0] : nma[0];
        lds_wait(L_CTL + C_CONV, 4);
        ldsw<f32x4>(xw + lane * 16, f32x4{os[0], os[1], os[2], os[3]}); ldsw<f32x4>(xw + 1024 + lane * 16, f32x4{os[4], os[5], os[6], os[7]});
        ldsw<f32x2>(xw + 2048 + lane * 8, f32x2{os[8], ms});
        STAMP(30); BAR();
        const u32x4 bv = __builtin_amdgcn_raw_buffer_load_b128(rs, WS_BT + (64 * oc + (tid >> 3)) * 4096 + ort * 128 + (tid & 7) * 16, 0, SC1);
        SBAR();
        const f32x4 p0 = ldsr<f32x4>(xr + lane * 16), p1 = ldsr<f32x4>(xr + 1024 + lane * 16); const f32x2 p2 = ldsr<f32x2>(xr + 2048 + lane * 8);
        const float mn = fminf(mk, p2[1]), f0 = EXP2(mn - mk), f1 = EXP2(mn - p2[1]);
        float l = o[8] * f0 + p2[0] * f1;
        const bool safe = o[8] < 0x1p60f && p2[0] < 0x1p60f && l > 0x1p-30f;
#pragma unroll
        for (int i = 0; i < 4; ++i) { o[i] = o[i] * f0 + p0[i] * f1; o[4 + i] = o[4 + i] * f0 + p1[i] * f1; }
        if (!__all(safe)) {
            const bf16x8 q = kh ? qb : qa; float m = -INFINITY; o = zero16;
            for (int t = 0; t < 64; ++t) {
                const unsigned so = kvh + t * KVT;
                const bf16x8 k = __builtin_bit_cast(bf16x8, __builtin_amdgcn_raw_buffer_load_b128(rs, voffK, so, SC1));
                const bf16x8 v0 = __builtin_bit_cast(bf16x8, __builtin_amdgcn_raw_buffer_load_b128(rs, voffV, so, SC1));
                const bf16x8 v1 = __builtin_bit_cast(bf16x8, __builtin_amdgcn_raw_buffer_load_b128(rs, voffV + 640, so, SC1));
                f32x16 s = MFMA32(k, q, zero16);
                const float m2 = fmaxf(m, rowmax(s)), al = EXP2(m - m2); m = m2;
#pragma unroll
                for (int i = 0; i < 16; ++i) { s[i] = EXP2(s[i] - m); o[i] *= al; }
                o = SMF(v0, lo8(s), o); o = SMF(v1, hi8(s), o);
            }
            l = o[8];
        }
        const float rl = 1.0f / l;
        __builtin_amdgcn_raw_buffer_store_b128(u32x4{pk2(o[0] * rl, o[1] * rl), pk2(o[2] * rl, o[3] * rl), pk2(o[4] * rl, o[5] * rl), pk2(o[6] * rl, o[7] * rl)}, rs,
                                               WS_OF + ((rt * 4 + 2 * kh + (r >> 4)) * 16 + 2 * hg + (hd >> 1)) * 1024 + (2 * (hd & 1) + hh) * 256 + r16 * 16, 0, SC1);
        ldsw<u32x4>(L_BI + (tid >> 3) * BROW + (tid & 7) * 16, bv);
    }
    VBAR(0);
    STAMP(9);
    if (wid == 0) {
        if (lane == 0) __hip_atomic_store((GAS u64*)(ws + WS_F3 + (rt * 8 + hg) * 256), tag, RLX_AGENT);
    }
    const int rg = wid & 3;
    bf16x8 wf[8][4];
#pragma unroll
    for (int m = 0; m < 4; ++m) {
        const unsigned a = (256 * kh + 16 * (g >> 1) + 4 * (g & 1) + (r16 >> 2)) * 128 + ((m ^ (2 * (g & 1) + (r16 >> 3))) << 5) + (lane & 3) * 8;
#pragma unroll
        for (int ks = 0; ks < 8; ++ks) wf[ks][m] = trfrag(a + ks * 4096, a + ks * 4096 + 1024);
    }
    if (wid == 0) {
        wait_flags((GAS u64*)(ws + WS_F3 + (ort * 8 + (lane & 7)) * 256), tag);
        if (bid == 0 && lane == 0) __hip_atomic_store((GAS u64*)(ws + WS_NONCE), tag, RLX_AGENT);
    }
    BAR();
    STAMP(10);
    {
        const float gate = __uint_as_float(__builtin_amdgcn_raw_buffer_load_b32(rs, WS_G + (ort * 64 + 16 * rg + r16) * 4, 0, SC1));
        bf16x8 of[8];
#pragma unroll
        for (int ks = 0; ks < 8; ++ks) of[ks] = __builtin_bit_cast(bf16x8, __builtin_amdgcn_raw_buffer_load_b128(rs, WS_OF + ((ort * 4 + rg) * 16 + 8 * kh + ks) * 1024 + lane * 16, 0, SC1));
        SBAR();
        STAMP(28);
        f32x4 acc[4];
#pragma unroll
        for (int m = 0; m < 4; ++m) acc[m] = f32x4{0.f, 0.f, 0.f, 0.f};
#pragma unroll
        for (int ks = 0; ks < 8; ++ks)
#pragma unroll
            for (int m = 0; m < 4; ++m) acc[m] = MFMA16(wf[ks][m], of[ks], acc[m]);
        STAMP(29);
        const f32x4 k0 = kh ? acc[2] : acc[0], k1 = kh ? acc[3] : acc[1], s0 = kh ? acc[0] : acc[2], s1 = kh ? acc[1] : acc[3];
        ldsw<f32x4>(L_RED + wid * 2048 + lane * 16, s0); ldsw<f32x4>(L_RED + wid * 2048 + 1024 + lane * 16, s1);
        BAR();
        STAMP(11);
        const float omg = rbf(1.0f - gate);
#pragma unroll
        for (int e = 0; e < 2; ++e) {
            const f32x4 a = (e ? k1 : k0) + ldsr<f32x4>(L_RED + (wid ^ 4) * 2048 + e * 1024 + lane * 16);
            const u32x2 b = trread(L_BI + (16 * (2 * kh + e) + 4 * g + (r16 >> 2)) * BROW + rg * 32 + (lane & 3) * 8);
            const unsigned a0 = pk2(a[0], a[1]), a1 = pk2(a[2], a[3]);
            const unsigned ga0 = pk2(gate * bf2f(a0 & 0xffff), gate * bfhi(a0)), ga1 = pk2(gate * bf2f(a1 & 0xffff), gate * bfhi(a1));
            const unsigned gb0 = pk2(omg * bf2f(b[0] & 0xffff), omg * bfhi(b[0])), gb1 = pk2(omg * bf2f(b[1] & 0xffff), omg * bfhi(b[1]));
            *(u32x2*)(P.out + (ort * 64 + 16 * rg + r16) * 512 + 64 * oc + 16 * (2 * kh + e) + 4 * g) =
                u32x2{pk2(bf2f(ga0 & 0xffff) + bf2f(gb0 & 0xffff), bfhi(ga0) + bfhi(gb0)), pk2(bf2f(ga1 & 0xffff) + bf2f(gb1 & 0xffff), bfhi(ga1) + bfhi(gb1))};
        }
    }
    STAMP(12); STAMP_OUT();
}

extern "C" void kernel_launch(void* const* d_in, const int* in_sizes, int n_in, void* d_out, int out_size, void* d_ws, size_t ws_size, hipStream_t stream) {
    Params p;
    p.x = (const u16*)d_in[0]; p.wqkv = (const u16*)d_in[1]; p.wo = (const u16*)d_in[2];
    p.are = (const u16*)d_in[4]; p.aim = (const u16*)d_in[5]; p.bre = (const u16*)d_in[6]; p.bim = (const u16*)d_in[7];
    p.cre = (const u16*)d_in[8]; p.cim = (const u16*)d_in[9]; p.logdt = (const u16*)d_in[10]; p.dskip = (const u16*)d_in[11]; p.gw = (const u16*)d_in[12];
    p.out = (u16*)d_out; p.ws = (char*)d_ws;
    (void)hipFuncSetAttribute((const void*)fused_kernel, hipFuncAttributeMaxDynamicSharedMemorySize, LDS_BYTES);
    fused_kernel<<<256, 512, LDS_BYTES, stream>>>(p);
}
```

```cpp
#include <hip/hip_runtime.h>
#include <hip/hip_bf16.h>
#include <stdint.h>

typedef float f32x2 __attribute__((ext_vector_type(2)));
typedef float f32x4 __attribute__((ext_vector_type(4)));
typedef float f32x8 __attribute__((ext_vector_type(8)));
typedef float f32x16 __attribute__((ext_vector_type(16)));
typedef __bf16 bf16x2 __attribute__((ext_vector_type(2)));
typedef __bf16 bf16x8 __attribute__((ext_vector_type(8)));
typedef __bf16 bf16x16 __attribute__((ext_vector_type(16)));
typedef short s16x4 __attribute__((ext_vector_type(4)));
typedef unsigned u32x2 __attribute__((ext_vector_type(2)));
typedef unsigned u32x4 __attribute__((ext_vector_type(4)));
typedef unsigned short u16;
typedef unsigned long long u64;
#define LAS __attribute__((address_space(3)))
#define GAS __attribute__((address_space(1)))
#define CAS __attribute__((address_space(4)))
#define RLX_AGENT __ATOMIC_RELAXED, __HIP_MEMORY_SCOPE_AGENT
#define SC1 16
#define NA 1
#define LTRIP 4
#define ELOW 7
#define ROT rt
#define CPRIO 3

constexpr unsigned WS_NONCE = 0, WS_F1 = 4096, WS_F2 = WS_F1 + 65536, WS_F3 = WS_F2 + 65536;
constexpr unsigned WS_G = WS_F3 + 65536;
constexpr unsigned WS_KV = 262144, KVT = 2304;
constexpr unsigned WS_XT = WS_KV + 32 * 64 * KVT;
constexpr unsigned WS_BT = WS_XT + 2097152;
constexpr unsigned WS_OF = WS_BT + 2097152;
constexpr unsigned WS_END = WS_OF + 2097152;
constexpr unsigned WS_STAMP = 16u << 20;
constexpr unsigned SLOT = 24576, TROW = 528;
constexpr unsigned slot_at(int c) { return ((c + 4) % 6) * SLOT; }
constexpr unsigned L_LO = 73728, LOC = 32 * TROW;
constexpr unsigned L_HI = 107520, HIC = 27 * TROW;
constexpr unsigned L_PAR = 147456;
constexpr unsigned L_CTL = 163712, LDS_BYTES = 163840;
constexpr unsigned L_QX = 65536;
constexpr unsigned KCP = 4224;
constexpr unsigned L_UB = 147456, UBC = 7680;
constexpr unsigned L_PA = 107520;
constexpr unsigned L_XB = 73728, XBW = 2560;
constexpr unsigned L_BI = 107520, BROW = 144, L_RED = 122880;
enum { C_KV = 0, C_WQ = 4, C_U = 8, C_CONV = 12, C_TAB = 16, C_R7 = 20, C_GO = 24, C_E = 32 };
constexpr float QSCALE = 0.25f * 1.4426950408889634f;
constexpr int SMF_IDX = (int)0xDDDDDDDD;

__device__ __forceinline__ float bf2f(unsigned v) { return __uint_as_float(v << 16); }
__device__ __forceinline__ float bfhi(unsigned v) { return __uint_as_float(v & 0xffff0000u); }
__device__ __forceinline__ unsigned pk2(float lo, float hi) { bf16x2 v = {(__bf16)lo, (__bf16)hi}; return __builtin_bit_cast(unsigned, v); }
__device__ __forceinline__ float rbf(float v) { return bf2f(__builtin_bit_cast(u16, (__bf16)v)); }
__device__ __forceinline__ u32x2 trread(unsigned a) { return __builtin_bit_cast(u32x2, __builtin_amdgcn_ds_read_tr16_b64_v4i16((LAS s16x4*)a)); }
__device__ __forceinline__ bf16x8 trfrag(unsigned a0, unsigned a1) { const u32x2 a = trread(a0), b = trread(a1); return __builtin_bit_cast(bf16x8, u32x4{a[0], a[1], b[0], b[1]}); }
template <typename T> __device__ __forceinline__ T ldsr(unsigned a) { return *(LAS const T*)a; }
template <typename T> __device__ __forceinline__ void ldsw(unsigned a, T v) { *(LAS T*)a = v; }
__device__ __forceinline__ void glds_s(const void* sbase, unsigned voff, unsigned lds) {
    asm volatile("s_mov_b32 m0, %2\n\ts_nop 0\n\tglobal_load_lds_dwordx4 %0, %1" :: "v"(voff), "s"(sbase), "s"(lds) : "memory");
}
__device__ __forceinline__ void glds_v(const void* vaddr, unsigned lds) {
    asm volatile("s_mov_b32 m0, %1\n\ts_nop 0\n\tglobal_load_lds_dwordx4 %0, off" :: "v"(vaddr), "s"(lds) : "memory");
}
#define BAR() asm volatile("s_waitcnt lgkmcnt(0)\n\ts_barrier" ::: "memory")
#define VBAR(n) asm volatile("s_waitcnt vmcnt(" #n ") lgkmcnt(0)\n\ts_barrier" ::: "memory")
#define SBAR() __builtin_amdgcn_sched_barrier(0)
#define MFMA32(a, b, c) __builtin_amdgcn_mfma_f32_32x32x16_bf16(a, b, c, 0, 0, 0)
#define MFMA16(a, b, c) __builtin_amdgcn_mfma_f32_16x16x32_bf16(a, b, c, 0, 0, 0)
#define SMF(a, p, o) __builtin_amdgcn_smfmac_f32_32x32x32_bf16(a, __builtin_bit_cast(bf16x16, p), o, SMF_IDX, 0, 0)
#define EXP2(v) __builtin_amdgcn_exp2f(v)
#define STAMP(i) do { if ((i) == 0) dg0 = __builtin_amdgcn_s_memrealtime(); if ((i) == 0) dgk = __builtin_amdgcn_s_memrealtime(); } while (0)
#define STAMP_DECL u64 dg0 = 0, dgk = 0
#define STAMP_OUT() do { const u64 lim = dg0 + 6000 + (dgk ? 4 * (dgk - dg0) : 0); for (int z = 0; z < 100000 && __builtin_amdgcn_s_memrealtime() < lim; ++z) __builtin_amdgcn_s_sleep(2); } while (0)

struct C2 { float r0, i0, r1, i1; };
__device__ __forceinline__ C2 cmul2(const C2& a, const C2& b) {
    C2 d;
    asm("v_mul_f32 %0, %4, %8\n\tv_mul_f32 %1, %5, %8\n\tv_mul_f32 %2, %6, %10\n\tv_mul_f32 %3, %7, %10\n\t"
        "v_fma_f32 %0, -%5, %9, %0\n\tv_fma_f32 %1, %4, %9, %1\n\tv_fma_f32 %2, -%7, %11, %2\n\tv_fma_f32 %3, %6, %11, %3"
        : "=&v"(d.r0), "=&v"(d.i0), "=&v"(d.r1), "=&v"(d.i1) : "v"(a.r0), "v"(a.i0), "v"(a.r1), "v"(a.i1), "v"(b.r0), "v"(b.i0), "v"(b.r1), "v"(b.i1));
    return d;
}
__device__ __forceinline__ u32x2 cpack(const C2& a) { return u32x2{pk2(a.r0, a.i0), pk2(a.r1, a.i1)}; }
__device__ __forceinline__ f32x2 cexpm(float al2, float rev, float mult) {
    const float e = EXP2(al2 * mult), f = __builtin_amdgcn_fractf(rev * mult);
    return f32x2{e * __builtin_amdgcn_cosf(f), e * __builtin_amdgcn_sinf(f)};
}
__device__ __forceinline__ C2 cexp2(float l0, float v0, float l1, float v1, float mult) {
    const f32x2 a = cexpm(l0, v0, mult), b = cexpm(l1, v1, mult); return C2{a[0], a[1], b[0], b[1]};
}
__device__ __forceinline__ void wait_flags(GAS u64* p, u64 tag) {
    for (int spins = 0; spins < (1 << 18); ++spins) {
        if (__all(__hip_atomic_load(p, RLX_AGENT) == tag)) break;
        __builtin_amdgcn_s_sleep(2);
    }
}
__device__ __forceinline__ f32x8 lo8(const f32x16& s) { return __builtin_shufflevector(s, s, 0, 1, 2, 3, 4, 5, 6, 7); }
__device__ __forceinline__ f32x8 hi8(const f32x16& s) { return __builtin_shufflevector(s, s, 8, 9, 10, 11, 12, 13, 14, 15); }
__device__ __forceinline__ float rowmax(const f32x16& s) {
    float m = fmaxf(fmaxf(s[0], s[1]), s[2]);
#pragma unroll
    for (int i = 3; i < 15; i += 2) m = fmaxf(fmaxf(m, s[i]), s[i + 1]);
    m = fmaxf(m, s[15]);
    const auto r = __builtin_amdgcn_permlane32_swap(__float_as_uint(m), __float_as_uint(m), false, false);
    return fmaxf(__uint_as_float(r[0]), __uint_as_float(r[1]));
}

struct Params {
    const u16 *x, *wqkv, *wo, *are, *aim, *bre, *bim, *cre, *cim, *logdt, *dskip, *gw;
    u16* out; char* ws;
};
__device__ __forceinline__ unsigned lds_add(unsigned a) { return __hip_atomic_fetch_add((LAS unsigned*)a, 1u, __ATOMIC_RELAXED, __HIP_MEMORY_SCOPE_WORKGROUP); }
__device__ __forceinline__ void lds_wait(unsigned a, unsigned n) {
    for (int sp = 0; sp < (1 << 22) && *(volatile LAS unsigned*)a < n; ++sp) __builtin_amdgcn_s_sleep(1);
    asm volatile("" ::: "memory");
}
__global__ void __launch_bounds__(512, 2) fused_kernel(Params P) {
    char* const ws = P.ws;
    const int tid = threadIdx.x, lane = tid & 63, wid = __builtin_amdgcn_readfirstlane(tid >> 6), bid = blockIdx.x;
    const int xcd = bid & 7, jb = bid >> 3;
    const int hg = (xcd % (8 / NA)) * NA + (jb % NA), rt = (xcd / (8 / NA)) * (32 / NA) + jb / NA;
    const int ort = 4 * xcd + (jb >> 3), oc = jb & 7;
    const int c0 = 64 * hg + 2 * rt;
    const int r = lane & 31, hh = lane >> 5, g = lane >> 4, r16 = lane & 15;
    STAMP_DECL; STAMP(0);
    asm volatile("" :: "s"(P.x), "s"(P.wqkv), "s"(P.are), "s"(P.aim), "s"(P.bre), "s"(P.bim), "s"(P.cre), "s"(P.cim), "s"(P.logdt), "s"(P.dskip), "s"(P.gw), "s"(P.ws), "s"(P.wo), "s"(P.out));
    const int tt = wid & 1, pp = (wid >> 1) & 1;
    f32x16 zero16;
#pragma unroll
    for (int i = 0; i < 16; ++i) zero16[i] = 0.f;
    const __amdgpu_buffer_rsrc_t rs = __builtin_amdgcn_make_buffer_rsrc(ws, 0, WS_END, 0x00020000);
    u64 tag;
    unsigned dk2;

    if (__builtin_expect(wid < 4, 1)) {
        const unsigned vx0 = (16 * wid + (lane >> 3)) * 1024 + (((lane & 7) ^ g) << 4), vx1 = vx0 + 8192 + ((((lane & 7) ^ g ^ 4) - ((lane & 7) ^ g)) << 4);
        const unsigned vw = (16 * wid + (lane >> 3)) * 3072 + (((((lane & 7) >> 1) ^ ((g & 1) << 1)) * 2 + (lane & 1)) << 4);
        const unsigned vw1 = vw + 24576;
        const char* xb = (const char*)P.x + rt * 65536;
        const char* wb = (const char*)P.wqkv + hg * 128;
        auto dmaW = [&](int m, int cc, unsigned dst) { const char* s = wb + m * 1024 + ((cc + hg) & 7) * 196608;
            glds_s(s, vw, dst + wid * 2048); glds_s(s, vw1, dst + wid * 2048 + 1024); };
        auto dmaC = [&](int cc) { const unsigned sl = slot_at(cc); const char* s = xb + ((cc + hg) & 7) * 128;
            glds_s(s, vx0, sl + wid * 2048); glds_s(s, vx1, sl + wid * 2048 + 1024); dmaW(1, cc, sl + 8192); dmaW(2, cc, sl + 16384); };
        dmaC(0);
        {
            const int a = wid < 3 ? wid : 2;
            const u16* s0 = a == 0 ? P.are : a == 1 ? P.bre : P.cre;
            const u16* s1 = a == 0 ? P.aim : a == 1 ? P.bim : P.cim;
            glds_v((const char*)(hh ? s1 : s0) + c0 * 256 + r * 16, L_PAR + a * 1024);
        }
        STAMP(13);
        dmaC(1); dmaC(2);
        STAMP(14);
        const unsigned ld2 = ((CAS const unsigned*)P.logdt)[c0 >> 1];
        dk2 = ((CAS const unsigned*)P.dskip)[c0 >> 1];
        const float dt = rbf(__expf(tt ? bfhi(ld2) : bf2f(ld2 & 0xffff)));
        u32x2 lv[16], hv[14];
#define PINV(v, n) _Pragma("unroll") for (int i_ = 0; i_ < (n); ++i_) asm volatile("" : "+v"(v[i_]))
        VBAR(12); STAMP(1); dmaC(3);
        const unsigned pb = L_PAR + tt * 256 + lane * 4;
        const unsigned pr = ldsr<unsigned>(pb), pi = ldsr<unsigned>(pb + 512), br = ldsr<unsigned>(pb + 1024), bi = ldsr<unsigned>(pb + 1536), cr = ldsr<unsigned>(pb + 2048), ci = ldsr<unsigned>(pb + 2560);
        VBAR(12); STAMP(16); dmaC(4);
        const float l0 = dt * bf2f(pr & 0xffff) * 1.4426950408889634f, l1 = dt * bfhi(pr) * 1.4426950408889634f;
        const float v0 = dt * bf2f(pi & 0xffff) * 0.15915494309189535f, v1 = dt * bfhi(pi) * 0.15915494309189535f;
        C2 e = cexp2(l0, v0, l1, v1, 8.f), a = cexp2(l0, v0, l1, v1, 128.f * pp);
#define LO(j0, j1) _Pragma("unroll") for (int j = (j0); j < (j1); ++j) { lv[j] = cpack(a); if (j < 15) a = cmul2(a, e); } PINV(lv, 16); SBAR()
        SBAR();
        VBAR(12); STAMP(17); dmaC(5); LO(0, 4);
        VBAR(12); STAMP(18); dmaC(6); LO(4, 8);
        VBAR(12); STAMP(19); dmaC(7); LO(8, 12);
        VBAR(12); STAMP(20); dmaW(0, 0, 0); dmaW(0, 1, 8192); dmaW(0, 2, 16384); LO(12, 16);
        VBAR(12); STAMP(21); dmaW(0, 3, 24576); dmaW(0, 4, 32768); dmaW(0, 5, 40960);
        a = cmul2(C2{bf2f(cr & 0xffff), -bf2f(ci & 0xffff), bfhi(cr), -bfhi(ci)}, C2{dt * bf2f(br & 0xffff), -dt * bf2f(bi & 0xffff), dt * bfhi(br), -dt * bfhi(bi)});
        const C2 gg = cexp2(l0, -v0, l1, -v1, 256.f);
        e = cexp2(l0, -v0, l1, -v1, pp ? 4.f : 1.f);
        SBAR();
        VBAR(12); STAMP(22); dmaW(0, 6, 49152); dmaW(0, 7, 57344);
        tag = __hip_atomic_load((GAS u64*)(ws + WS_NONCE), RLX_AGENT) + 1;
        hv[13] = u32x2{0, 0};
        if (pp == 0) {
#pragma unroll
            for (int j = 0; j < 9; ++j) { if (j < 8) hv[j] = cpack(a); if (!(j & 1)) hv[8 + (j >> 1)] = cpack(cmul2(a, gg)); if (j < 8) a = cmul2(a, e); }
            PINV(hv, 13);
        } else {
            const C2 f = cmul2(e, e);
#pragma unroll
            for (int k = 1; k < 8; ++k) { a = cmul2(a, gg); if (k >= 2) { const int row = k < 4 ? 3 * (k - 2) : 6 + 2 * (k - 4); hv[row] = cpack(a);
                if (k < 4) hv[row + 1] = cpack(cmul2(a, e)); hv[row + (k < 4 ? 2 : 1)] = cpack(cmul2(a, f)); } }
            PINV(hv, 14);
        }
        STAMP(2);
        asm volatile("s_waitcnt vmcnt(0)" ::: "memory");
        if (lane == 0) lds_add(L_CTL + C_WQ);
        lds_wait(L_CTL + C_R7, 4);
        STAMP(3);
        const unsigned tl = L_LO + tt * LOC + 16 * pp * TROW + lane * 8, th = L_HI + tt * HIC + 13 * pp * TROW + lane * 8;
#pragma unroll
        for (int i = 0; i < 16; ++i) { ldsw<u32x2>(tl + i * TROW, lv[i]); if (i < 13 || (i == 13 && pp)) ldsw<u32x2>(th + i * TROW, hv[i]); }
        asm volatile("s_waitcnt lgkmcnt(0)" ::: "memory");
        if (lane == 0) lds_add(L_CTL + C_TAB);
        STAMP(23);
        if (wid == 3) {
            wait_flags((GAS u64*)(ws + WS_F1 + (hg * 32 + r) * 256), tag);
            if (lane == 0) lds_add(L_CTL + C_GO);
        } else if (wid == 2) {
            lds_wait(L_CTL + C_TAB, 4);
#pragma unroll
            for (int i = 0; i < 5; ++i) ldsw<u32x4>(L_UB + hh * UBC + (r + 32 * i) * 16, u32x4{0, 0, 0, 0});
        } else {
            const int c = wid;
            const u64 mp = r < 8 ? 0x0706050403020100ull : r < 16 ? 0x121110100b0a0908ull : r < 24 ? 0x000000120f0e0d0cull : 0x1a19181716151413ull;
            const unsigned aa = L_HI + c * HIC + (unsigned)((mp >> ((r & 7) * 8)) & 0xff) * TROW + hh * 16, ba = L_LO + c * LOC + r * TROW + hh * 16;
            lds_wait(L_CTL + C_TAB, 4);
            bf16x8 ta[16], tb[16];
#pragma unroll
            for (int s = 0; s < 4; ++s) { ta[s] = ldsr<bf16x8>(aa + s * 32); tb[s] = ldsr<bf16x8>(ba + s * 32); }
            SBAR();
            f32x16 k = zero16;
#pragma unroll
            for (int s = 0; s < 16; ++s) { k = MFMA32(ta[s], tb[s], k); SBAR(); if (s < 12) { ta[s + 4] = ldsr<bf16x8>(aa + s * 32 + 128); tb[s + 4] = ldsr<bf16x8>(ba + s * 32 + 128); } SBAR(); }
            STAMP(15);
            asm volatile("" ::: "memory");
            const unsigned K0 = L_LO + c * LOC, R = K0 + (2040 - 8 * r) * 2;
            ldsw<u32x2>(R + (1 - hh) * 8, u32x2{pk2(k[3], k[2]), pk2(k[1], k[0])});
            ldsw<u32x4>(R - (32 + 64 * hh) * 16, u32x4{pk2(k[8], k[7]), pk2(k[6], k[6]), pk2(k[6], k[5]), pk2(k[4], k[4])});
            if (hh == 0) ldsw<u32x4>(R - 64 * 16, u32x4{pk2(k[11], k[11]), pk2(k[10], k[10]), pk2(k[10], k[9]), pk2(k[9], k[9])});
#pragma unroll
            for (int e_ = 0; e_ < 2; ++e_) { const float a_ = k[12 + 2 * e_], b_ = k[13 + 2 * e_]; const unsigned aa2 = pk2(a_, a_);
                ldsw<u32x4>(R - (128 + 32 * e_ + 64 * hh) * 16, u32x4{pk2(b_, b_), pk2(b_, (r & 1) ? b_ : a_), aa2, aa2}); }
            if (lane < 8) ldsw<u32x4>(K0 + 4096 + lane * 16, u32x4{0, 0, 0, 0});
            asm volatile("" ::: "memory");
            u32x4 cu[5]; u32x2 pv[5];
#pragma unroll
            for (int ps = 0; ps < 5; ++ps) { const unsigned a_ = K0 + (ps < 4 ? 64 * ps + lane : 256 + (lane & 7)) * 16; cu[ps] = ldsr<u32x4>(a_); pv[ps] = ldsr<u32x2>(a_ - 8); }
#pragma unroll
            for (int ps = 0; ps < 5; ++ps) {
                const unsigned am = __builtin_amdgcn_alignbit(pv[ps][1], pv[ps][0], 16), a0 = __builtin_amdgcn_alignbit(cu[ps][0], pv[ps][1], 16), a1 = __builtin_amdgcn_alignbit(cu[ps][1], cu[ps][0], 16),
                               a2 = __builtin_amdgcn_alignbit(cu[ps][2], cu[ps][1], 16), a3 = __builtin_amdgcn_alignbit(cu[ps][3], cu[ps][2], 16);
                const unsigned W = K0 + KCP + (ps < 4 ? 64 * ps + lane : 256 + (lane & 7)) * 16;
                ldsw<u32x4>(W, u32x4{a0, a1, a2, a3}); ldsw<u32x4>(W + KCP, u32x4{pv[ps][1], cu[ps][0], cu[ps][1], cu[ps][2]}); ldsw<u32x4>(W + 2 * KCP, u32x4{am, a0, a1, a2});
            }
        }
    } else {
        if (wid == 4 && lane < 16) ldsw<unsigned>(L_CTL + lane * 4, 0u);
        __builtin_amdgcn_s_setprio(2);
        const int w4 = wid - 4;
        tag = __hip_atomic_load((GAS u64*)(ws + WS_NONCE), RLX_AGENT) + 1;
        {
            const int grow = rt * 64 + 8 * hg + 2 * w4;
            const u32x4 gv = *(const u32x4*)(P.gw + lane * 8), x0 = *(const u32x4*)(P.x + grow * 512 + lane * 8), x1 = *(const u32x4*)(P.x + grow * 512 + 512 + lane * 8);
            float s0 = 0.f, s1 = 0.f;
#pragma unroll
            for (int j = 0; j < 4; ++j) { const unsigned gj = gv[j], aj = x0[j], bj = x1[j];
                s0 = __builtin_amdgcn_fdot2_f32_bf16(__builtin_bit_cast(bf16x2, aj), __builtin_bit_cast(bf16x2, gj), s0, false);
                s1 = __builtin_amdgcn_fdot2_f32_bf16(__builtin_bit_cast(bf16x2, bj), __builtin_bit_cast(bf16x2, gj), s1, false);
            }
            const auto sa = __builtin_amdgcn_permlane32_swap(__float_as_uint(s0), __float_as_uint(s1), false, false);
            const unsigned t0 = __float_as_uint(__uint_as_float(sa[0]) + __uint_as_float(sa[1]));
            const auto sc = __builtin_amdgcn_permlane16_swap(t0, t0, false, false);
            float t = __uint_as_float(sc[0]) + __uint_as_float(sc[1]);
            t += __uint_as_float(__builtin_amdgcn_update_dpp(0u, __float_as_uint(t), 0x128, 0xf, 0xf, false));
            t += __uint_as_float(__builtin_amdgcn_update_dpp(0u, __float_as_uint(t), 0x124, 0xf, 0xf, false));
            t += __uint_as_float(__builtin_amdgcn_update_dpp(0u, __float_as_uint(t), 0x122, 0xf, 0xf, false));
            t += __uint_as_float(__builtin_amdgcn_update_dpp(0u, __float_as_uint(t), 0x121, 0xf, 0xf, false));
            const float gate = rbf(1.0f / (1.0f + __expf(-rbf(t))));
            if (r == 0) __builtin_amdgcn_raw_buffer_store_b32(__float_as_uint(gate), rs, WS_G + (grow + hh) * 4, 0, SC1);
        }
        dk2 = ((CAS const unsigned*)P.dskip)[c0 >> 1];
        unsigned xa[2][4], wk[2];
#pragma unroll
        for (int ks = 0; ks < 4; ++ks) { xa[0][ks] = (32 * tt + r) * 128 + (((2 * ks + hh) ^ ((r >> 1) & 7)) << 4); xa[1][ks] = xa[0][ks] + 3 * SLOT; asm volatile("" : "+v"(xa[1][ks])); }
        const unsigned wa = (8 * hh + (r16 >> 2)) * 128 + (((2 * pp + (g & 1)) ^ (r16 >> 3 << 1)) << 5) + (lane & 3) * 8;
        wk[0] = wa + 8192; wk[1] = wa + 8192 + 3 * SLOT; asm volatile("" : "+v"(wk[0]), "+v"(wk[1]));
        f32x16 ak = zero16, av = zero16;
        bf16x8 xq[32];
        u32x2 fk[4][2], fv[4][2];
#define SH(cc) (slot_at(cc) >= 3 * SLOT)
#define SO(cc) (slot_at(cc) % (3 * SLOT))
#define RX(cc, ks) xq[4 * (cc) + (ks)] = ldsr<bf16x8>(xa[SH(cc)][ks] + SO(cc))
#define RK(cc, ks, t) fk[ks][t] = trread(wk[SH(cc)] + SO(cc) + (ks) * 2048 + (t) * 512)
#define RV(cc, ks, t) fv[ks][t] = trread(wk[SH(cc)] + SO(cc) + 8192 + (ks) * 2048 + (t) * 512)
#define FR(f, ks) __builtin_bit_cast(bf16x8, u32x4{f[ks][0][0], f[ks][0][1], f[ks][1][0], f[ks][1][1]})
#define MK(cc, ks) ak = MFMA32(FR(fk, ks), xq[4 * (cc) + (ks)], ak); SBAR()
#define MV(cc, ks) av = MFMA32(xq[4 * (cc) + (ks)], FR(fv, ks), av); SBAR()
#pragma unroll
        for (int cc = 0; cc < 8; ++cc) {
            STAMP(14 + cc); BAR();
            if (cc) { MK(cc - 1, 2); } RX(cc, 0); RK(cc, 0, 0); RK(cc, 0, 1); SBAR();
            if (cc) { MV(cc - 1, 2); } RV(cc, 0, 0); RV(cc, 0, 1); RX(cc, 1); RK(cc, 1, 0); SBAR();
            if (cc) { MK(cc - 1, 3); } RK(cc, 1, 1); RV(cc, 1, 0); RV(cc, 1, 1); SBAR();
            if (cc) { MV(cc - 1, 3); } RX(cc, 2); RK(cc, 2, 0); RK(cc, 2, 1); RV(cc, 2, 0); SBAR();
            MK(cc, 0); RV(cc, 2, 1); RX(cc, 3); RK(cc, 3, 0); SBAR();
            MV(cc, 0); RK(cc, 3, 1); RV(cc, 3, 0); RV(cc, 3, 1); SBAR();
            if (cc == 0) {
                u32x2 t[4];
#pragma unroll
                for (int n = 0; n < 4; ++n) { const int row = 16 * g + 4 * n + (r16 >> 2);
                    t[n] = trread(slot_at(0) + row * 128 + (((2 * w4 + ((lane & 3) >> 1)) ^ ((row >> 1) & 7)) << 4) + (lane & 1) * 8); }
                MK(cc, 1); MV(cc, 1);
                const unsigned o = WS_XT + (64 * hg + 16 * w4 + r16) * 4096 + rt * 128 + g * 32;
                __builtin_amdgcn_raw_buffer_store_b128(u32x4{t[0][0], t[0][1], t[1][0], t[1][1]}, rs, o, 0, SC1);
                __builtin_amdgcn_raw_buffer_store_b128(u32x4{t[2][0], t[2][1], t[3][0], t[3][1]}, rs, o + 16, 0, SC1);
                SBAR();
            } else { MK(cc, 1); MV(cc, 1); }
        }
        MK(7, 2); MV(7, 2); MK(7, 3); MV(7, 3);
        STAMP(1);
        {
            const unsigned tb = WS_KV + ((4 * hg + 2 * pp) * 64 + 2 * rt + tt) * KVT;
            const int rp = (r & 19) | ((r & 4) << 1) | ((r & 8) >> 1);
            const unsigned vb = tb + (r >> 4) * (64 * KVT) + 1024;
#pragma unroll
            for (int e = 0; e < 2; ++e) {
                __builtin_amdgcn_raw_buffer_store_b128(u32x4{pk2(ak[8 * e], ak[8 * e + 1]), pk2(ak[8 * e + 2], ak[8 * e + 3]), pk2(ak[8 * e + 4], ak[8 * e + 5]), pk2(ak[8 * e + 6], ak[8 * e + 7])},
                                                       rs, tb + e * (64 * KVT) + hh * 512 + rp * 16, 0, SC1);
                __builtin_amdgcn_raw_buffer_store_b128(u32x4{pk2(av[8 * e], av[8 * e + 1]), pk2(av[8 * e + 2], av[8 * e + 3]), pk2(av[8 * e + 4], av[8 * e + 5]), pk2(av[8 * e + 6], av[8 * e + 7])},
                                                       rs, vb + e * 640 + hh * 256 + r16 * 16, 0, SC1);
            }
            if (r16 == 0) __builtin_amdgcn_raw_buffer_store_b128(u32x4{0x3f803f80u, 0x3f803f80u, 0x3f803f80u, 0x3f803f80u}, rs, vb + hh * 640 + 512, 0, SC1);
        }
        if (lane == 0) lds_add(L_CTL + C_R7);
        STAMP(2);
        asm volatile("s_waitcnt vmcnt(0) lgkmcnt(0)" ::: "memory");
        if (lane == 0 && lds_add(L_CTL + C_KV) == 3u) __hip_atomic_store((GAS u64*)(ws + WS_F1 + (hg * 32 + rt) * 256), tag, RLX_AGENT);
        STAMP(3);
        lds_wait(L_CTL + C_WQ, 4);
        STAMP(23);
        f32x16 aq = zero16;
        bf16x8 wq[8];
#pragma unroll
        for (int ks = 0; ks < 8; ++ks) wq[ks] = trfrag(ks * 2048 + wa, ks * 2048 + wa + 512);
        SBAR();
#pragma unroll
        for (int i = 0; i < 32; ++i) {
            aq = MFMA32(wq[i & 7], xq[i], aq); SBAR();
            if (i < 24) wq[i & 7] = trfrag((i + 8) * 2048 + wa, (i + 8) * 2048 + wa + 512);
            SBAR();
        }
#pragma unroll
        for (int e_ = 0; e_ < 2; ++e_)
            ldsw<u32x4>(L_QX + ((2 * pp + e_) * 2 + tt) * 1024 + lane * 16,
                        u32x4{pk2(aq[8 * e_] * QSCALE, aq[8 * e_ + 1] * QSCALE), pk2(aq[8 * e_ + 2] * QSCALE, aq[8 * e_ + 3] * QSCALE),
                              pk2(aq[8 * e_ + 4] * QSCALE, aq[8 * e_ + 5] * QSCALE), pk2(aq[8 * e_ + 6] * QSCALE, aq[8 * e_ + 7] * QSCALE)});
        asm volatile("s_waitcnt lgkmcnt(0)" ::: "memory");
        if (lane == 0) lds_add(L_CTL + C_GO);
    }
    STAMP(24);
    lds_wait(L_CTL + C_GO, 5);
    STAMP(4);
    const int hd = wid & 3, kh = wid >> 2;
    if (kh) __builtin_amdgcn_s_setprio(2); else __builtin_amdgcn_s_setprio(3);
    const unsigned voffK = lane * 16, voffV = r < 16 ? 1024 + hh * 256 + r * 16 : (r == 16 || r == 20) ? 1536 : 0x4000000;
    const unsigned kvh = WS_KV + (4 * hg + hd) * 64 * KVT;
    unsigned vK[4], vV[4];
#pragma unroll
    for (int u = 0; u < 4; ++u) { vK[u] = voffK + u * KVT; vV[u] = voffV + u * KVT; asm volatile("" : "+v"(vK[u]), "+v"(vV[u])); }
    auto tsoff = [&](int tr) -> unsigned { return tr < 8 ? kvh + (32 * kh + 4 * ((tr + ROT) & 7)) * KVT : WS_END; };
    bf16x8 kf[4], va[4], vb[4];
#define LOADT(u, so_) do { \
        va[u] = __builtin_bit_cast(bf16x8, __builtin_amdgcn_raw_buffer_load_b128(rs, vV[u], so_, SC1)); \
        vb[u] = __builtin_bit_cast(bf16x8, __builtin_amdgcn_raw_buffer_load_b128(rs, vV[u] + 640, so_, SC1)); \
        kf[u] = __builtin_bit_cast(bf16x8, __builtin_amdgcn_raw_buffer_load_b128(rs, vK[u], so_, SC1)); } while (0)
    unsigned soc = tsoff(0);
    u32x4 uv[2];
    if (!kh) {
#pragma unroll
        for (int c = 0; c < 2; ++c) uv[c] = __builtin_amdgcn_raw_buffer_load_b128(rs, WS_XT + (c0 + c) * 4096 + tid * 16, 0, SC1);
    }
    LOADT(0, soc); LOADT(1, soc); LOADT(2, soc);
    SBAR();
    vb[3] = __builtin_bit_cast(bf16x8, u32x4{0, 0, 0, 0});
    const bf16x8 qa = ldsr<bf16x8>(L_QX + hd * 2048 + lane * 16), qb = ldsr<bf16x8>(L_QX + hd * 2048 + 1024 + lane * 16);
    if (!kh) {
#pragma unroll
        for (int c = 0; c < 2; ++c) ldsw<u32x4>(L_UB + c * UBC + (32 + (tid >> 2)) * 80 + (tid & 3) * 16, uv[c]);
        asm volatile("s_waitcnt lgkmcnt(0)" ::: "memory");
        if (lane == 0) lds_add(L_CTL + C_U);
    }
    f32x16 nma, nmb, oa = zero16, ob = zero16, sa, sb = zero16;
    { const float ma = -rowmax(MFMA32(kf[0], qa, zero16)), mb = -rowmax(MFMA32(kf[0], qb, zero16));
#pragma unroll
      for (int i = 0; i < 16; ++i) { nma[i] = ma; nmb[i] = mb; } }
    sa = MFMA32(kf[0], qa, nma);
    STAMP(5);
#define EX(s, a, b) _Pragma("unroll") for (int i_ = (a); i_ < (b); ++i_) s[i_] = EXP2(s[i_]); SBAR()
    auto attn = [&](int t0, int t1) {
        for (int tr = t0; tr < t1; ++tr) {
            const unsigned son = tsoff(tr + 1);
            if (tr == ELOW && !kh) {
                __builtin_amdgcn_s_setprio(0);
                if (wid == 0) wait_flags((GAS u64*)(ws + WS_F2 + (oc * 32 + r) * 256), tag);
            }
#pragma unroll
            for (int u = 0; u < 4; ++u) {
                EX(sa, 0, 2); ob = SMF(vb[(u + 3) & 3], hi8(sb), ob); SBAR(); LOADT((u + 3) & 3, u ? son : soc); SBAR();
                EX(sa, 2, 5); sb = MFMA32(kf[u], qb, nmb); SBAR();
                EX(sa, 5, 11); oa = SMF(va[u], lo8(sa), oa); SBAR();
                EX(sa, 11, 16);
                EX(sb, 0, 2); oa = SMF(vb[u], hi8(sa), oa); SBAR();
                EX(sb, 2, 5); sa = MFMA32(kf[(u + 1) & 3], qa, nma); SBAR();
                EX(sb, 5, 11); ob = SMF(va[u], lo8(sb), ob); SBAR();
                EX(sb, 11, 16);
            }
            soc = son;
        }
    };
    const int ctrip = kh ? LTRIP : 0;
    attn(0, ctrip);
    STAMP(6);
    {
        __builtin_amdgcn_s_setprio(CPRIO);
        const int c = wid & 1, e = (wid >> 1) & 1, dlt = 16 * kh + 8 * e + 7;
        const int kap = (r + 1) & 3;
        unsigned ap = L_LO + c * LOC + kap * KCP + (2047 - r + kap + 8 * hh - 32 * dlt) * 2 - 2048;
        unsigned bp = L_UB + c * UBC + (32 + r - dlt) * 80 + hh * 16;
        const unsigned pA = L_PA + (c * 2 + e) * 8192 + lane * 16;
        f32x16 y0 = zero16, y1 = zero16;
        lds_wait(L_CTL + C_U, 4);
        if (kh) {
            lds_wait(L_CTL + C_E + 4 * (c * 2 + e), 1);
#pragma unroll
            for (int i = 0; i < 4; ++i) { const f32x4 p = ldsr<f32x4>(pA + i * 1024), q = ldsr<f32x4>(pA + 4096 + i * 1024);
#pragma unroll
                for (int k = 0; k < 4; ++k) { y0[4 * i + k] = p[k]; y1[4 * i + k] = q[k]; } }
        }
        struct Fr { u32x2 a0, a1, z0, z1; bf16x8 b0, b1; } f[2];
#define CA(i) (ap + 32 * (i))
#define CB(i) (bp + 80 * ((i) >> 1) + 32 * ((i) & 1))
#define CAT(x, y) __builtin_bit_cast(bf16x8, u32x4{x[0], x[1], y[0], y[1]})
        f[0].a0 = ldsr<u32x2>(CA(0) + 2048); f[0].a1 = ldsr<u32x2>(CA(0) + 2056); f[0].b0 = ldsr<bf16x8>(CB(0));
        f[0].b1 = ldsr<bf16x8>(CB(0) + 2560); f[0].z0 = ldsr<u32x2>(CA(0)); f[0].z1 = ldsr<u32x2>(CA(0) + 8);
        SBAR();
#pragma unroll
        for (int i = 0; i < 16; ++i) {
            Fr& q = f[i & 1]; Fr& n = f[(i + 1) & 1];
            y0 = MFMA32(CAT(q.a0, q.a1), q.b0, y0); SBAR();
            if (i < 15) { n.a0 = ldsr<u32x2>(CA(i + 1) + 2048); n.a1 = ldsr<u32x2>(CA(i + 1) + 2056); n.b0 = ldsr<bf16x8>(CB(i + 1)); } SBAR();
            y1 = MFMA32(CAT(q.a0, q.a1), q.b1, y1); SBAR();
            if (i < 15) { n.b1 = ldsr<bf16x8>(CB(i + 1) + 2560); n.z0 = ldsr<u32x2>(CA(i + 1)); n.z1 = ldsr<u32x2>(CA(i + 1) + 8); } SBAR();
            y1 = MFMA32(CAT(q.z0, q.z1), q.b0, y1); SBAR();
        }
        STAMP(27);
#pragma unroll
        for (int i = 0; i < 4; ++i) {
            if (!kh || e) ldsw<f32x4>(pA + i * 1024, f32x4{y0[4 * i], y0[4 * i + 1], y0[4 * i + 2], y0[4 * i + 3]});
            if (!kh || !e) ldsw<f32x4>(pA + 4096 + i * 1024, f32x4{y1[4 * i], y1[4 * i + 1], y1[4 * i + 2], y1[4 * i + 3]});
        }
        asm volatile("s_waitcnt lgkmcnt(0)" ::: "memory");
        if (lane == 0) lds_add(L_CTL + C_E + 4 * (c * 2 + e));
        if (kh) {
            lds_wait(L_CTL + C_E + 4 * (c * 2 + 1 - e), 2);
            f32x16 y = e ? y1 : y0;
            const unsigned pO = L_PA + (c * 2 + 1 - e) * 8192 + e * 4096 + lane * 16;
#pragma unroll
            for (int i = 0; i < 4; ++i) { const f32x4 p = ldsr<f32x4>(pO + i * 1024);
#pragma unroll
                for (int k = 0; k < 4; ++k) y[4 * i + k] += p[k]; }
            const float dsk = c ? bfhi(dk2) : bf2f(dk2 & 0xffff);
            u32x2 w[4];
#pragma unroll
            for (int i = 0; i < 4; ++i) {
                const u32x2 u = ldsr<u32x2>(L_UB + c * UBC + (32 + 32 * e + r) * 80 + i * 16 + hh * 8);
                const unsigned q0 = pk2(y[4 * i], y[4 * i + 1]), q1 = pk2(y[4 * i + 2], y[4 * i + 3]);
                const unsigned s0 = pk2(bf2f(u[0] & 0xffff) * dsk, bfhi(u[0]) * dsk), s1 = pk2(bf2f(u[1] & 0xffff) * dsk, bfhi(u[1]) * dsk);
                w[i] = u32x2{pk2(bf2f(q0 & 0xffff) + bf2f(s0 & 0xffff), bfhi(q0) + bfhi(s0)), pk2(bf2f(q1 & 0xffff) + bf2f(s1 & 0xffff), bfhi(q1) + bfhi(s1))};
            }
#pragma unroll
            for (int i = 0; i < 4; i += 2) {
                const auto s0 = __builtin_amdgcn_permlane32_swap(w[i][0], w[i + 1][0], false, false), s1 = __builtin_amdgcn_permlane32_swap(w[i][1], w[i + 1][1], false, false);
                __builtin_amdgcn_raw_buffer_store_b128(u32x4{s0[0], s1[0], s0[1], s1[1]}, rs, WS_BT + (c0 + c) * 4096 + (32 * e + r) * 64 + i * 16 + hh * 16, 0, SC1);
            }
            asm volatile("s_waitcnt vmcnt(0) lgkmcnt(0)" ::: "memory");
            if (lane == 0 && lds_add(L_CTL + C_CONV) == 3u) __hip_atomic_store((GAS u64*)(ws + WS_F2 + (hg * 32 + rt) * 256), tag, RLX_AGENT);
        } else {
            const unsigned vo = (128 * wid + (lane >> 3)) * 1024 + (((((lane & 7) >> 1) ^ g) * 2 + (lane & 1)) << 4);
            const char* s = (const char*)P.wo + oc * 128;
#pragma unroll
            for (int i = 0; i < 16; ++i) glds_s(s + i * 8192, vo, wid * 16384 + i * 1024);
        }
        __builtin_amdgcn_s_setprio(1);
    }
    STAMP(7);
    attn(ctrip, 8);
    ob = SMF(vb[3], hi8(sb), ob);
    STAMP(8);
    {
        const unsigned xw = L_XB + wid * XBW, xr = L_XB + (wid ^ 4) * XBW;
        const f32x16 os = kh ? oa : ob; f32x16 o = kh ? ob : oa;
        const float ms = kh ? nma[0] : nmb[0], mk = kh ? nmb[0] : nma[0];
        lds_wait(L_CTL + C_CONV, 4);
        ldsw<f32x4>(xw + lane * 16, f32x4{os[0], os[1], os[2], os[3]}); ldsw<f32x4>(xw + 1024 + lane * 16, f32x4{os[4], os[5], os[6], os[7]});
        ldsw<f32x2>(xw + 2048 + lane * 8, f32x2{os[8], ms});
        STAMP(30); BAR();
        const u32x4 bv = __builtin_amdgcn_raw_buffer_load_b128(rs, WS_BT + (64 * oc + (tid >> 3)) * 4096 + ort * 128 + (tid & 7) * 16, 0, SC1);
        SBAR();
        const f32x4 p0 = ldsr<f32x4>(xr + lane * 16), p1 = ldsr<f32x4>(xr + 1024 + lane * 16); const f32x2 p2 = ldsr<f32x2>(xr + 2048 + lane * 8);
        const float mn = fminf(mk, p2[1]), f0 = EXP2(mn - mk), f1 = EXP2(mn - p2[1]);
        float l = o[8] * f0 + p2[0] * f1;
        const bool safe = o[8] < 0x1p60f && p2[0] < 0x1p60f && l > 0x1p-30f;
#pragma unroll
        for (int i = 0; i < 4; ++i) { o[i] = o[i] * f0 + p0[i] * f1; o[4 + i] = o[4 + i] * f0 + p1[i] * f1; }
        if (!__all(safe)) {
            const bf16x8 q = kh ? qb : qa; float m = -INFINITY; o = zero16;
            for (int t = 0; t < 64; ++t) {
                const unsigned so = kvh + t * KVT;
                const bf16x8 k = __builtin_bit_cast(bf16x8, __builtin_amdgcn_raw_buffer_load_b128(rs, voffK, so, SC1));
                const bf16x8 v0 = __builtin_bit_cast(bf16x8, __builtin_amdgcn_raw_buffer_load_b128(rs, voffV, so, SC1));
                const bf16x8 v1 = __builtin_bit_cast(bf16x8, __builtin_amdgcn_raw_buffer_load_b128(rs, voffV + 640, so, SC1));
                f32x16 s = MFMA32(k, q, zero16);
                const float m2 = fmaxf(m, rowmax(s)), al = EXP2(m - m2); m = m2;
#pragma unroll
                for (int i = 0; i < 16; ++i) { s[i] = EXP2(s[i] - m); o[i] *= al; }
                o = SMF(v0, lo8(s), o); o = SMF(v1, hi8(s), o);
            }
            l = o[8];
        }
        const float rl = 1.0f / l;
        __builtin_amdgcn_raw_buffer_store_b128(u32x4{pk2(o[0] * rl, o[1] * rl), pk2(o[2] * rl, o[3] * rl), pk2(o[4] * rl, o[5] * rl), pk2(o[6] * rl, o[7] * rl)}, rs,
                                               WS_OF + ((rt * 4 + 2 * kh + (r >> 4)) * 16 + 2 * hg + (hd >> 1)) * 1024 + (2 * (hd & 1) + hh) * 256 + r16 * 16, 0, SC1);
        ldsw<u32x4>(L_BI + (tid >> 3) * BROW + (tid & 7) * 16, bv);
    }
    VBAR(0);
    STAMP(9);
    if (wid == 0) {
        if (lane == 0) __hip_atomic_store((GAS u64*)(ws + WS_F3 + (rt * 8 + hg) * 256), tag, RLX_AGENT);
    }
    const int rg = wid & 3;
    bf16x8 wf[8][4];
#pragma unroll
    for (int m = 0; m < 4; ++m) {
        const unsigned a = (256 * kh + 16 * (g >> 1) + 4 * (g & 1) + (r16 >> 2)) * 128 + ((m ^ (2 * (g & 1) + (r16 >> 3))) << 5) + (lane & 3) * 8;
#pragma unroll
        for (int ks = 0; ks < 8; ++ks) wf[ks][m] = trfrag(a + ks * 4096, a + ks * 4096 + 1024);
    }
    if (wid == 0) {
        wait_flags((GAS u64*)(ws + WS_F3 + (ort * 8 + (lane & 7)) * 256), tag);
        if (bid == 0 && lane == 0) __hip_atomic_store((GAS u64*)(ws + WS_NONCE), tag, RLX_AGENT);
    }
    BAR();
    STAMP(10);
    {
        const float gate = __uint_as_float(__builtin_amdgcn_raw_buffer_load_b32(rs, WS_G + (ort * 64 + 16 * rg + r16) * 4, 0, SC1));
        bf16x8 of[8];
#pragma unroll
        for (int ks = 0; ks < 8; ++ks) of[ks] = __builtin_bit_cast(bf16x8, __builtin_amdgcn_raw_buffer_load_b128(rs, WS_OF + ((ort * 4 + rg) * 16 + 8 * kh + ks) * 1024 + lane * 16, 0, SC1));
        SBAR();
        STAMP(28);
        f32x4 acc[4];
#pragma unroll
        for (int m = 0; m < 4; ++m) acc[m] = f32x4{0.f, 0.f, 0.f, 0.f};
#pragma unroll
        for (int ks = 0; ks < 8; ++ks)
#pragma unroll
            for (int m = 0; m < 4; ++m) acc[m] = MFMA16(wf[ks][m], of[ks], acc[m]);
        STAMP(29);
        const f32x4 k0 = kh ? acc[2] : acc[0], k1 = kh ? acc[3] : acc[1], s0 = kh ? acc[0] : acc[2], s1 = kh ? acc[1] : acc[3];
        ldsw<f32x4>(L_RED + wid * 2048 + lane * 16, s0); ldsw<f32x4>(L_RED + wid * 2048 + 1024 + lane * 16, s1);
        BAR();
        STAMP(11);
        const float omg = rbf(1.0f - gate);
#pragma unroll
        for (int e = 0; e < 2; ++e) {
            const f32x4 a = (e ? k1 : k0) + ldsr<f32x4>(L_RED + (wid ^ 4) * 2048 + e * 1024 + lane * 16);
            const u32x2 b = trread(L_BI + (16 * (2 * kh + e) + 4 * g + (r16 >> 2)) * BROW + rg * 32 + (lane & 3) * 8);
            const unsigned a0 = pk2(a[0], a[1]), a1 = pk2(a[2], a[3]);
            const unsigned ga0 = pk2(gate * bf2f(a0 & 0xffff), gate * bfhi(a0)), ga1 = pk2(gate * bf2f(a1 & 0xffff), gate * bfhi(a1));
            const unsigned gb0 = pk2(omg * bf2f(b[0] & 0xffff), omg * bfhi(b[0])), gb1 = pk2(omg * bf2f(b[1] & 0xffff), omg * bfhi(b[1]));
            *(u32x2*)(P.out + (ort * 64 + 16 * rg + r16) * 512 + 64 * oc + 16 * (2 * kh + e) + 4 * g) =
                u32x2{pk2(bf2f(ga0 & 0xffff) + bf2f(gb0 & 0xffff), bfhi(ga0) + bfhi(gb0)), pk2(bf2f(ga1 & 0xffff) + bf2f(gb1 & 0xffff), bfhi(ga1) + bfhi(gb1))};
        }
    }
    STAMP(12); STAMP_OUT();
}

extern "C" void kernel_launch(void* const* d_in, const int* in_sizes, int n_in, void* d_out, int out_size, void* d_ws, size_t ws_size, hipStream_t stream) {
    Params p;
    p.x = (const u16*)d_in[0]; p.wqkv = (const u16*)d_in[1]; p.wo = (const u16*)d_in[2];
    p.are = (const u16*)d_in[4]; p.aim = (const u16*)d_in[5]; p.bre = (const u16*)d_in[6]; p.bim = (const u16*)d_in[7];
    p.cre = (const u16*)d_in[8]; p.cim = (const u16*)d_in[9]; p.logdt = (const u16*)d_in[10]; p.dskip = (const u16*)d_in[11]; p.gw = (const u16*)d_in[12];
    p.out = (u16*)d_out; p.ws = (char*)d_ws;
    (void)hipFuncSetAttribute((const void*)fused_kernel, hipFuncAttributeMaxDynamicSharedMemorySize, LDS_BYTES);
    fused_kernel<<<256, 512, LDS_BYTES, stream>>>(p);
}
```

```cpp
#include <hip/hip_runtime.h>
#include <hip/hip_bf16.h>
#include <stdint.h>

typedef float f32x2 __attribute__((ext_vector_type(2)));
typedef float f32x4 __attribute__((ext_vector_type(4)));
typedef float f32x8 __attribute__((ext_vector_type(8)));
typedef float f32x16 __attribute__((ext_vector_type(16)));
typedef __bf16 bf16x2 __attribute__((ext_vector_type(2)));
typedef __bf16 bf16x8 __attribute__((ext_vector_type(8)));
typedef __bf16 bf16x16 __attribute__((ext_vector_type(16)));
typedef short s16x4 __attribute__((ext_vector_type(4)));
typedef unsigned u32x2 __attribute__((ext_vector_type(2)));
typedef unsigned u32x4 __attribute__((ext_vector_type(4)));
typedef unsigned short u16;
typedef unsigned long long u64;
#define LAS __attribute__((address_space(3)))
#define GAS __attribute__((address_space(1)))
#define CAS __attribute__((address_space(4)))
#define RLX_AGENT __ATOMIC_RELAXED, __HIP_MEMORY_SCOPE_AGENT
#define SC1 16
#define NA 1
#define LTRIP 4
#define ELOW 7
#define ROT rt
#define CPRIO 3

constexpr unsigned WS_NONCE = 0, WS_F1 = 4096, WS_F2 = WS_F1 + 65536, WS_F3 = WS_F2 + 65536;
constexpr unsigned WS_G = WS_F3 + 65536;
constexpr unsigned WS_KV = 262144, KVT = 2304;
constexpr unsigned WS_XT = WS_KV + 32 * 64 * KVT;
constexpr unsigned WS_BT = WS_XT + 2097152;
constexpr unsigned WS_OF = WS_BT + 2097152;
constexpr unsigned WS_END = WS_OF + 2097152;
constexpr unsigned WS_STAMP = 16u << 20;
constexpr unsigned SLOT = 24576, TROW = 528;
constexpr unsigned slot_at(int c) { return ((c + 4) % 6) * SLOT; }
constexpr unsigned L_LO = 73728, LOC = 32 * TROW;
constexpr unsigned L_HI = 107520, HIC = 27 * TROW;
constexpr unsigned L_PAR = 147456;
constexpr unsigned L_CTL = 163712, LDS_BYTES = 163840;
constexpr unsigned L_QX = 65536;
constexpr unsigned KCP = 4224;
constexpr unsigned L_UB = 147456, UBC = 7680;
constexpr unsigned L_PA = 107520;
constexpr unsigned L_XB = 73728, XBW = 2560;
constexpr unsigned L_BI = 107520, BROW = 144, L_RED = 122880;
enum { C_KV = 0, C_WQ = 4, C_U = 8, C_CONV = 12, C_TAB = 16, C_R7 = 20, C_GO = 24, C_E = 32 };
constexpr float QSCALE = 0.25f * 1.4426950408889634f;
constexpr int SMF_IDX = (int)0xDDDDDDDD;

__device__ __forceinline__ float bf2f(unsigned v) { return __uint_as_float(v << 16); }
__device__ __forceinline__ float bfhi(unsigned v) { return __uint_as_float(v & 0xffff0000u); }
__device__ __forceinline__ unsigned pk2(float lo, float hi) { bf16x2 v = {(__bf16)lo, (__bf16)hi}; return __builtin_bit_cast(unsigned, v); }
__device__ __forceinline__ float rbf(float v) { return bf2f(__builtin_bit_cast(u16, (__bf16)v)); }
__device__ __forceinline__ u32x2 trread(unsigned a) { return __builtin_bit_cast(u32x2, __builtin_amdgcn_ds_read_tr16_b64_v4i16((LAS s16x4*)a)); }
__device__ __forceinline__ bf16x8 trfrag(unsigned a0, unsigned a1) { const u32x2 a = trread(a0), b = trread(a1); return __builtin_bit_cast(bf16x8, u32x4{a[0], a[1], b[0], b[1]}); }
template <typename T> __device__ __forceinline__ T ldsr(unsigned a) { return *(LAS const T*)a; }
template <typename T> __device__ __forceinline__ void ldsw(unsigned a, T v) { *(LAS T*)a = v; }
__device__ __forceinline__ void glds_s(const void* sbase, unsigned voff, unsigned lds) {
    asm volatile("s_mov_b32 m0, %2\n\ts_nop 0\n\tglobal_load_lds_dwordx4 %0, %1" :: "v"(voff), "s"(sbase), "s"(lds) : "memory");
}
__device__ __forceinline__ void glds_v(const void* vaddr, unsigned lds) {
    asm volatile("s_mov_b32 m0, %1\n\ts_nop 0\n\tglobal_load_lds_dwordx4 %0, off" :: "v"(vaddr), "s"(lds) : "memory");
}
#define BAR() asm volatile("s_waitcnt lgkmcnt(0)\n\ts_barrier" ::: "memory")
#define VBAR(n) asm volatile("s_waitcnt vmcnt(" #n ") lgkmcnt(0)\n\ts_barrier" ::: "memory")
#define SBAR() __builtin_amdgcn_sched_barrier(0)
#define MFMA32(a, b, c) __builtin_amdgcn_mfma_f32_32x32x16_bf16(a, b, c, 0, 0, 0)
#define MFMA16(a, b, c) __builtin_amdgcn_mfma_f32_16x16x32_bf16(a, b, c, 0, 0, 0)
#define SMF(a, p, o) __builtin_amdgcn_smfmac_f32_32x32x32_bf16(a, __builtin_bit_cast(bf16x16, p), o, SMF_IDX, 0, 0)
#define EXP2(v) __builtin_amdgcn_exp2f(v)
#define STAMP(i) do { if ((i) == 0) asm volatile("s_memrealtime %0\n\ts_waitcnt lgkmcnt(0)" : "=s"(dg0)); } while (0)
#define STAMP_DECL u64 dg0 = 0
#define STAMP_OUT() do { const u64 pe = __hip_atomic_load((GAS u64*)(ws + WS_STAMP), RLX_AGENT); const u64 lim = pe + 6000 + 0 * (dg0 - pe); for (int z = 0; z < 100000 && __builtin_amdgcn_s_memrealtime() < lim; ++z) __builtin_amdgcn_s_sleep(2); } while (0)

struct C2 { float r0, i0, r1, i1; };
__device__ __forceinline__ C2 cmul2(const C2& a, const C2& b) {
    C2 d;
    asm("v_mul_f32 %0, %4, %8\n\tv_mul_f32 %1, %5, %8\n\tv_mul_f32 %2, %6, %10\n\tv_mul_f32 %3, %7, %10\n\t"
        "v_fma_f32 %0, -%5, %9, %0\n\tv_fma_f32 %1, %4, %9, %1\n\tv_fma_f32 %2, -%7, %11, %2\n\tv_fma_f32 %3, %6, %11, %3"
        : "=&v"(d.r0), "=&v"(d.i0), "=&v"(d.r1), "=&v"(d.i1) : "v"(a.r0), "v"(a.i0), "v"(a.r1), "v"(a.i1), "v"(b.r0), "v"(b.i0), "v"(b.r1), "v"(b.i1));
    return d;
}
__device__ __forceinline__ u32x2 cpack(const C2& a) { return u32x2{pk2(a.r0, a.i0), pk2(a.r1, a.i1)}; }
__device__ __forceinline__ f32x2 cexpm(float al2, float rev, float mult) {
    const float e = EXP2(al2 * mult), f = __builtin_amdgcn_fractf(rev * mult);
    return f32x2{e * __builtin_amdgcn_cosf(f), e * __builtin_amdgcn_sinf(f)};
}
__device__ __forceinline__ C2 cexp2(float l0, float v0, float l1, float v1, float mult) {
    const f32x2 a = cexpm(l0, v0, mult), b = cexpm(l1, v1, mult); return C2{a[0], a[1], b[0], b[1]};
}
__device__ __forceinline__ void wait_flags(GAS u64* p, u64 tag) {
    for (int spins = 0; spins < (1 << 18); ++spins) {
        if (__all(__hip_atomic_load(p, RLX_AGENT) == tag)) break;
        __builtin_amdgcn_s_sleep(2);
    }
}
__device__ __forceinline__ f32x8 lo8(const f32x16& s) { return __builtin_shufflevector(s, s, 0, 1, 2, 3, 4, 5, 6, 7); }
__device__ __forceinline__ f32x8 hi8(const f32x16& s) { return __builtin_shufflevector(s, s, 8, 9, 10, 11, 12, 13, 14, 15); }
__device__ __forceinline__ float rowmax(const f32x16& s) {
    float m = fmaxf(fmaxf(s[0], s[1]), s[2]);
#pragma unroll
    for (int i = 3; i < 15; i += 2) m = fmaxf(fmaxf(m, s[i]), s[i + 1]);
    m = fmaxf(m, s[15]);
    const auto r = __builtin_amdgcn_permlane32_swap(__float_as_uint(m), __float_as_uint(m), false, false);
    return fmaxf(__uint_as_float(r[0]), __uint_as_float(r[1]));
}

struct Params {
    const u16 *x, *wqkv, *wo, *are, *aim, *bre, *bim, *cre, *cim, *logdt, *dskip, *gw;
    u16* out; char* ws;
};
__device__ __forceinline__ unsigned lds_add(unsigned a) { return __hip_atomic_fetch_add((LAS unsigned*)a, 1u, __ATOMIC_RELAXED, __HIP_MEMORY_SCOPE_WORKGROUP); }
__device__ __forceinline__ void lds_wait(unsigned a, unsigned n) {
    for (int sp = 0; sp < (1 << 22) && *(volatile LAS unsigned*)a < n; ++sp) __builtin_amdgcn_s_sleep(1);
    asm volatile("" ::: "memory");
}
__global__ void __launch_bounds__(512, 2) fused_kernel(Params P) {
    STAMP_DECL; STAMP(0);
    char* const ws = P.ws;
    const int tid = threadIdx.x, lane = tid & 63, wid = __builtin_amdgcn_readfirstlane(tid >> 6), bid = blockIdx.x;
    const int xcd = bid & 7, jb = bid >> 3;
    const int hg = (xcd % (8 / NA)) * NA + (jb % NA), rt = (xcd / (8 / NA)) * (32 / NA) + jb / NA;
    const int ort = 4 * xcd + (jb >> 3), oc = jb & 7;
    const int c0 = 64 * hg + 2 * rt;
    const int r = lane & 31, hh = lane >> 5, g = lane >> 4, r16 = lane & 15;
    asm volatile("" :: "s"(P.x), "s"(P.wqkv), "s"(P.are), "s"(P.aim), "s"(P.bre), "s"(P.bim), "s"(P.cre), "s"(P.cim), "s"(P.logdt), "s"(P.dskip), "s"(P.gw), "s"(P.ws), "s"(P.wo), "s"(P.out));
    const int tt = wid & 1, pp = (wid >> 1) & 1;
    f32x16 zero16;
#pragma unroll
    for (int i = 0; i < 16; ++i) zero16[i] = 0.f;
    const __amdgpu_buffer_rsrc_t rs = __builtin_amdgcn_make_buffer_rsrc(ws, 0, WS_END, 0x00020000);
    u64 tag;
    unsigned dk2;

    if (__builtin_expect(wid < 4, 1)) {
        const unsigned vx0 = (16 * wid + (lane >> 3)) * 1024 + (((lane & 7) ^ g) << 4), vx1 = vx0 + 8192 + ((((lane & 7) ^ g ^ 4) - ((lane & 7) ^ g)) << 4);
        const unsigned vw = (16 * wid + (lane >> 3)) * 3072 + (((((lane & 7) >> 1) ^ ((g & 1) << 1)) * 2 + (lane & 1)) << 4);
        const unsigned vw1 = vw + 24576;
        const char* xb = (const char*)P.x + rt * 65536;
        const char* wb = (const char*)P.wqkv + hg * 128;
        auto dmaW = [&](int m, int cc, unsigned dst) { const char* s = wb + m * 1024 + ((cc + hg) & 7) * 196608;
            glds_s(s, vw, dst + wid * 2048); glds_s(s, vw1, dst + wid * 2048 + 1024); };
        auto dmaC = [&](int cc) { const unsigned sl = slot_at(cc); const char* s = xb + ((cc + hg) & 7) * 128;
            glds_s(s, vx0, sl + wid * 2048); glds_s(s, vx1, sl + wid * 2048 + 1024); dmaW(1, cc, sl + 8192); dmaW(2, cc, sl + 16384); };
        dmaC(0);
        {
            const int a = wid < 3 ? wid : 2;
            const u16* s0 = a == 0 ? P.are : a == 1 ? P.bre : P.cre;
            const u16* s1 = a == 0 ? P.aim : a == 1 ? P.bim : P.cim;
            glds_v((const char*)(hh ? s1 : s0) + c0 * 256 + r * 16, L_PAR + a * 1024);
        }
        STAMP(13);
        dmaC(1); dmaC(2);
        STAMP(14);
        const unsigned ld2 = ((CAS const unsigned*)P.logdt)[c0 >> 1];
        dk2 = ((CAS const unsigned*)P.dskip)[c0 >> 1];
        const float dt = rbf(__expf(tt ? bfhi(ld2) : bf2f(ld2 & 0xffff)));
        u32x2 lv[16], hv[14];
#define PINV(v, n) _Pragma("unroll") for (int i_ = 0; i_ < (n); ++i_) asm volatile("" : "+v"(v[i_]))
        VBAR(12); STAMP(1); dmaC(3);
        const unsigned pb = L_PAR + tt * 256 + lane * 4;
        const unsigned pr = ldsr<unsigned>(pb), pi = ldsr<unsigned>(pb + 512), br = ldsr<unsigned>(pb + 1024), bi = ldsr<unsigned>(pb + 1536), cr = ldsr<unsigned>(pb + 2048), ci = ldsr<unsigned>(pb + 2560);
        VBAR(12); STAMP(16); dmaC(4);
        const float l0 = dt * bf2f(pr & 0xffff) * 1.4426950408889634f, l1 = dt * bfhi(pr) * 1.4426950408889634f;
        const float v0 = dt * bf2f(pi & 0xffff) * 0.15915494309189535f, v1 = dt * bfhi(pi) * 0.15915494309189535f;
        C2 e = cexp2(l0, v0, l1, v1, 8.f), a = cexp2(l0, v0, l1, v1, 128.f * pp);
#define LO(j0, j1) _Pragma("unroll") for (int j = (j0); j < (j1); ++j) { lv[j] = cpack(a); if (j < 15) a = cmul2(a, e); } PINV(lv, 16); SBAR()
        SBAR();
        VBAR(12); STAMP(17); dmaC(5); LO(0, 4);
        VBAR(12); STAMP(18); dmaC(6); LO(4, 8);
        VBAR(12); STAMP(19); dmaC(7); LO(8, 12);
        VBAR(12); STAMP(20); dmaW(0, 0, 0); dmaW(0, 1, 8192); dmaW(0, 2, 16384); LO(12, 16);
        VBAR(12); STAMP(21); dmaW(0, 3, 24576); dmaW(0, 4, 32768); dmaW(0, 5, 40960);
        a = cmul2(C2{bf2f(cr & 0xffff), -bf2f(ci & 0xffff), bfhi(cr), -bfhi(ci)}, C2{dt * bf2f(br & 0xffff), -dt * bf2f(bi & 0xffff), dt * bfhi(br), -dt * bfhi(bi)});
        const C2 gg = cexp2(l0, -v0, l1, -v1, 256.f);
        e = cexp2(l0, -v0, l1, -v1, pp ? 4.f : 1.f);
        SBAR();
        VBAR(12); STAMP(22); dmaW(0, 6, 49152); dmaW(0, 7, 57344);
        tag = __hip_atomic_load((GAS u64*)(ws + WS_NONCE), RLX_AGENT) + 1;
        hv[13] = u32x2{0, 0};
        if (pp == 0) {
#pragma unroll
            for (int j = 0; j < 9; ++j) { if (j < 8) hv[j] = cpack(a); if (!(j & 1)) hv[8 + (j >> 1)] = cpack(cmul2(a, gg)); if (j < 8) a = cmul2(a, e); }
            PINV(hv, 13);
        } else {
            const C2 f = cmul2(e, e);
#pragma unroll
            for (int k = 1; k < 8; ++k) { a = cmul2(a, gg); if (k >= 2) { const int row = k < 4 ? 3 * (k - 2) : 6 + 2 * (k - 4); hv[row] = cpack(a);
                if (k < 4) hv[row + 1] = cpack(cmul2(a, e)); hv[row + (k < 4 ? 2 : 1)] = cpack(cmul2(a, f)); } }
            PINV(hv, 14);
        }
        STAMP(2);
        asm volatile("s_waitcnt vmcnt(0)" ::: "memory");
        if (lane == 0) lds_add(L_CTL + C_WQ);
        lds_wait(L_CTL + C_R7, 4);
        STAMP(3);
        const unsigned tl = L_LO + tt * LOC + 16 * pp * TROW + lane * 8, th = L_HI + tt * HIC + 13 * pp * TROW + lane * 8;
#pragma unroll
        for (int i = 0; i < 16; ++i) { ldsw<u32x2>(tl + i * TROW, lv[i]); if (i < 13 || (i == 13 && pp)) ldsw<u32x2>(th + i * TROW, hv[i]); }
        asm volatile("s_waitcnt lgkmcnt(0)" ::: "memory");
        if (lane == 0) lds_add(L_CTL + C_TAB);
        STAMP(23);
        if (wid == 3) {
            wait_flags((GAS u64*)(ws + WS_F1 + (hg * 32 + r) * 256), tag);
            if (lane == 0) lds_add(L_CTL + C_GO);
        } else if (wid == 2) {
            lds_wait(L_CTL + C_TAB, 4);
#pragma unroll
            for (int i = 0; i < 5; ++i) ldsw<u32x4>(L_UB + hh * UBC + (r + 32 * i) * 16, u32x4{0, 0, 0, 0});
        } else {
            const int c = wid;
            const u64 mp = r < 8 ? 0x0706050403020100ull : r < 16 ? 0x121110100b0a0908ull : r < 24 ? 0x000000120f0e0d0cull : 0x1a19181716151413ull;
            const unsigned aa = L_HI + c * HIC + (unsigned)((mp >> ((r & 7) * 8)) & 0xff) * TROW + hh * 16, ba = L_LO + c * LOC + r * TROW + hh * 16;
            lds_wait(L_CTL + C_TAB, 4);
            bf16x8 ta[16], tb[16];
#pragma unroll
            for (int s = 0; s < 4; ++s) { ta[s] = ldsr<bf16x8>(aa + s * 32); tb[s] = ldsr<bf16x8>(ba + s * 32); }
            SBAR();
            f32x16 k = zero16;
#pragma unroll
            for (int s = 0; s < 16; ++s) { k = MFMA32(ta[s], tb[s], k); SBAR(); if (s < 12) { ta[s + 4] = ldsr<bf16x8>(aa + s * 32 + 128); tb[s + 4] = ldsr<bf16x8>(ba + s * 32 + 128); } SBAR(); }
            STAMP(15);
            asm volatile("" ::: "memory");
            const unsigned K0 = L_LO + c * LOC, R = K0 + (2040 - 8 * r) * 2;
            ldsw<u32x2>(R + (1 - hh) * 8, u32x2{pk2(k[3], k[2]), pk2(k[1], k[0])});
            ldsw<u32x4>(R - (32 + 64 * hh) * 16, u32x4{pk2(k[8], k[7]), pk2(k[6], k[6]), pk2(k[6], k[5]), pk2(k[4], k[4])});
            if (hh == 0) ldsw<u32x4>(R - 64 * 16, u32x4{pk2(k[11], k[11]), pk2(k[10], k[10]), pk2(k[10], k[9]), pk2(k[9], k[9])});
#pragma unroll
            for (int e_ = 0; e_ < 2; ++e_) { const float a_ = k[12 + 2 * e_], b_ = k[13 + 2 * e_]; const unsigned aa2 = pk2(a_, a_);
                ldsw<u32x4>(R - (128 + 32 * e_ + 64 * hh) * 16, u32x4{pk2(b_, b_), pk2(b_, (r & 1) ? b_ : a_), aa2, aa2}); }
            if (lane < 8) ldsw<u32x4>(K0 + 4096 + lane * 16, u32x4{0, 0, 0, 0});
            asm volatile("" ::: "memory");
            u32x4 cu[5]; u32x2 pv[5];
#pragma unroll
            for (int ps = 0; ps < 5; ++ps) { const unsigned a_ = K0 + (ps < 4 ? 64 * ps + lane : 256 + (lane & 7)) * 16; cu[ps] = ldsr<u32x4>(a_); pv[ps] = ldsr<u32x2>(a_ - 8); }
#pragma unroll
            for (int ps = 0; ps < 5; ++ps) {
                const unsigned am = __builtin_amdgcn_alignbit(pv[ps][1], pv[ps][0], 16), a0 = __builtin_amdgcn_alignbit(cu[ps][0], pv[ps][1], 16), a1 = __builtin_amdgcn_alignbit(cu[ps][1], cu[ps][0], 16),
                               a2 = __builtin_amdgcn_alignbit(cu[ps][2], cu[ps][1], 16), a3 = __builtin_amdgcn_alignbit(cu[ps][3], cu[ps][2], 16);
                const unsigned W = K0 + KCP + (ps < 4 ? 64 * ps + lane : 256 + (lane & 7)) * 16;
                ldsw<u32x4>(W, u32x4{a0, a1, a2, a3}); ldsw<u32x4>(W + KCP, u32x4{pv[ps][1], cu[ps][0], cu[ps][1], cu[ps][2]}); ldsw<u32x4>(W + 2 * KCP, u32x4{am, a0, a1, a2});
            }
        }
    } else {
        if (wid == 4 && lane < 16) ldsw<unsigned>(L_CTL + lane * 4, 0u);
        __builtin_amdgcn_s_setprio(2);
        const int w4 = wid - 4;
        tag = __hip_atomic_load((GAS u64*)(ws + WS_NONCE), RLX_AGENT) + 1;
        {
            const int grow = rt * 64 + 8 * hg + 2 * w4;
            const u32x4 gv = *(const u32x4*)(P.gw + lane * 8), x0 = *(const u32x4*)(P.x + grow * 512 + lane * 8), x1 = *(const u32x4*)(P.x + grow * 512 + 512 + lane * 8);
            float s0 = 0.f, s1 = 0.f;
#pragma unroll
            for (int j = 0; j < 4; ++j) { const unsigned gj = gv[j], aj = x0[j], bj = x1[j];
                s0 = __builtin_amdgcn_fdot2_f32_bf16(__builtin_bit_cast(bf16x2, aj), __builtin_bit_cast(bf16x2, gj), s0, false);
                s1 = __builtin_amdgcn_fdot2_f32_bf16(__builtin_bit_cast(bf16x2, bj), __builtin_bit_cast(bf16x2, gj), s1, false);
            }
            const auto sa = __builtin_amdgcn_permlane32_swap(__float_as_uint(s0), __float_as_uint(s1), false, false);
            const unsigned t0 = __float_as_uint(__uint_as_float(sa[0]) + __uint_as_float(sa[1]));
            const auto sc = __builtin_amdgcn_permlane16_swap(t0, t0, false, false);
            float t = __uint_as_float(sc[0]) + __uint_as_float(sc[1]);
            t += __uint_as_float(__builtin_amdgcn_update_dpp(0u, __float_as_uint(t), 0x128, 0xf, 0xf, false));
            t += __uint_as_float(__builtin_amdgcn_update_dpp(0u, __float_as_uint(t), 0x124, 0xf, 0xf, false));
            t += __uint_as_float(__builtin_amdgcn_update_dpp(0u, __float_as_uint(t), 0x122, 0xf, 0xf, false));
            t += __uint_as_float(__builtin_amdgcn_update_dpp(0u, __float_as_uint(t), 0x121, 0xf, 0xf, false));
            const float gate = rbf(1.0f / (1.0f + __expf(-rbf(t))));
            if (r == 0) __builtin_amdgcn_raw_buffer_store_b32(__float_as_uint(gate), rs, WS_G + (grow + hh) * 4, 0, SC1);
        }
        dk2 = ((CAS const unsigned*)P.dskip)[c0 >> 1];
        unsigned xa[2][4], wk[2];
#pragma unroll
        for (int ks = 0; ks < 4; ++ks) { xa[0][ks] = (32 * tt + r) * 128 + (((2 * ks + hh) ^ ((r >> 1) & 7)) << 4); xa[1][ks] = xa[0][ks] + 3 * SLOT; asm volatile("" : "+v"(xa[1][ks])); }
        const unsigned wa = (8 * hh + (r16 >> 2)) * 128 + (((2 * pp + (g & 1)) ^ (r16 >> 3 << 1)) << 5) + (lane & 3) * 8;
        wk[0] = wa + 8192; wk[1] = wa + 8192 + 3 * SLOT; asm volatile("" : "+v"(wk[0]), "+v"(wk[1]));
        f32x16 ak = zero16, av = zero16;
        bf16x8 xq[32];
        u32x2 fk[4][2], fv[4][2];
#define SH(cc) (slot_at(cc) >= 3 * SLOT)
#define SO(cc) (slot_at(cc) % (3 * SLOT))
#define RX(cc, ks) xq[4 * (cc) + (ks)] = ldsr<bf16x8>(xa[SH(cc)][ks] + SO(cc))
#define RK(cc, ks, t) fk[ks][t] = trread(wk[SH(cc)] + SO(cc) + (ks) * 2048 + (t) * 512)
#define RV(cc, ks, t) fv[ks][t] = trread(wk[SH(cc)] + SO(cc) + 8192 + (ks) * 2048 + (t) * 512)
#define FR(f, ks) __builtin_bit_cast(bf16x8, u32x4{f[ks][0][0], f[ks][0][1], f[ks][1][0], f[ks][1][1]})
#define MK(cc, ks) ak = MFMA32(FR(fk, ks), xq[4 * (cc) + (ks)], ak); SBAR()
#define MV(cc, ks) av = MFMA32(xq[4 * (cc) + (ks)], FR(fv, ks), av); SBAR()
#pragma unroll
        for (int cc = 0; cc < 8; ++cc) {
            STAMP(14 + cc); BAR();
            if (cc) { MK(cc - 1, 2); } RX(cc, 0); RK(cc, 0, 0); RK(cc, 0, 1); SBAR();
            if (cc) { MV(cc - 1, 2); } RV(cc, 0, 0); RV(cc, 0, 1); RX(cc, 1); RK(cc, 1, 0); SBAR();
            if (cc) { MK(cc - 1, 3); } RK(cc, 1, 1); RV(cc, 1, 0); RV(cc, 1, 1); SBAR();
            if (cc) { MV(cc - 1, 3); } RX(cc, 2); RK(cc, 2, 0); RK(cc, 2, 1); RV(cc, 2, 0); SBAR();
            MK(cc, 0); RV(cc, 2, 1); RX(cc, 3); RK(cc, 3, 0); SBAR();
            MV(cc, 0); RK(cc, 3, 1); RV(cc, 3, 0); RV(cc, 3, 1); SBAR();
            if (cc == 0) {
                u32x2 t[4];
#pragma unroll
                for (int n = 0; n < 4; ++n) { const int row = 16 * g + 4 * n + (r16 >> 2);
                    t[n] = trread(slot_at(0) + row * 128 + (((2 * w4 + ((lane & 3) >> 1)) ^ ((row >> 1) & 7)) << 4) + (lane & 1) * 8); }
                MK(cc, 1); MV(cc, 1);
                const unsigned o = WS_XT + (64 * hg + 16 * w4 + r16) * 4096 + rt * 128 + g * 32;
                __builtin_amdgcn_raw_buffer_store_b128(u32x4{t[0][0], t[0][1], t[1][0], t[1][1]}, rs, o, 0, SC1);
                __builtin_amdgcn_raw_buffer_store_b128(u32x4{t[2][0], t[2][1], t[3][0], t[3][1]}, rs, o + 16, 0, SC1);
                SBAR();
            } else { MK(cc, 1); MV(cc, 1); }
        }
        MK(7, 2); MV(7, 2); MK(7, 3); MV(7, 3);
        STAMP(1);
        {
            const unsigned tb = WS_KV + ((4 * hg + 2 * pp) * 64 + 2 * rt + tt) * KVT;
            const int rp = (r & 19) | ((r & 4) << 1) | ((r & 8) >> 1);
            const unsigned vb = tb + (r >> 4) * (64 * KVT) + 1024;
#pragma unroll
            for (int e = 0; e < 2; ++e) {
                __builtin_amdgcn_raw_buffer_store_b128(u32x4{pk2(ak[8 * e], ak[8 * e + 1]), pk2(ak[8 * e + 2], ak[8 * e + 3]), pk2(ak[8 * e + 4], ak[8 * e + 5]), pk2(ak[8 * e + 6], ak[8 * e + 7])},
                                                       rs, tb + e * (64 * KVT) + hh * 512 + rp * 16, 0, SC1);
                __builtin_amdgcn_raw_buffer_store_b128(u32x4{pk2(av[8 * e], av[8 * e + 1]), pk2(av[8 * e + 2], av[8 * e + 3]), pk2(av[8 * e + 4], av[8 * e + 5]), pk2(av[8 * e + 6], av[8 * e + 7])},
                                                       rs, vb + e * 640 + hh * 256 + r16 * 16, 0, SC1);
            }
            if (r16 == 0) __builtin_amdgcn_raw_buffer_store_b128(u32x4{0x3f803f80u, 0x3f803f80u, 0x3f803f80u, 0x3f803f80u}, rs, vb + hh * 640 + 512, 0, SC1);
        }
        if (lane == 0) lds_add(L_CTL + C_R7);
        STAMP(2);
        asm volatile("s_waitcnt vmcnt(0) lgkmcnt(0)" ::: "memory");
        if (lane == 0 && lds_add(L_CTL + C_KV) == 3u) __hip_atomic_store((GAS u64*)(ws + WS_F1 + (hg * 32 + rt) * 256), tag, RLX_AGENT);
        STAMP(3);
        lds_wait(L_CTL + C_WQ, 4);
        STAMP(23);
        f32x16 aq = zero16;
        bf16x8 wq[8];
#pragma unroll
        for (int ks = 0; ks < 8; ++ks) wq[ks] = trfrag(ks * 2048 + wa, ks * 2048 + wa + 512);
        SBAR();
#pragma unroll
        for (int i = 0; i < 32; ++i) {
            aq = MFMA32(wq[i & 7], xq[i], aq); SBAR();
            if (i < 24) wq[i & 7] = trfrag((i + 8) * 2048 + wa, (i + 8) * 2048 + wa + 512);
            SBAR();
        }
#pragma unroll
        for (int e_ = 0; e_ < 2; ++e_)
            ldsw<u32x4>(L_QX + ((2 * pp + e_) * 2 + tt) * 1024 + lane * 16,
                        u32x4{pk2(aq[8 * e_] * QSCALE, aq[8 * e_ + 1] * QSCALE), pk2(aq[8 * e_ + 2] * QSCALE, aq[8 * e_ + 3] * QSCALE),
                              pk2(aq[8 * e_ + 4] * QSCALE, aq[8 * e_ + 5] * QSCALE), pk2(aq[8 * e_ + 6] * QSCALE, aq[8 * e_ + 7] * QSCALE)});
        asm volatile("s_waitcnt lgkmcnt(0)" ::: "memory");
        if (lane == 0) lds_add(L_CTL + C_GO);
    }
    STAMP(24);
    lds_wait(L_CTL + C_GO, 5);
    STAMP(4);
    const int hd = wid & 3, kh = wid >> 2;
    if (kh) __builtin_amdgcn_s_setprio(2); else __builtin_amdgcn_s_setprio(3);
    const unsigned voffK = lane * 16, voffV = r < 16 ? 1024 + hh * 256 + r * 16 : (r == 16 || r == 20) ? 1536 : 0x4000000;
    const unsigned kvh = WS_KV + (4 * hg + hd) * 64 * KVT;
    unsigned vK[4], vV[4];
#pragma unroll
    for (int u = 0; u < 4; ++u) { vK[u] = voffK + u * KVT; vV[u] = voffV + u * KVT; asm volatile("" : "+v"(vK[u]), "+v"(vV[u])); }
    auto tsoff = [&](int tr) -> unsigned { return tr < 8 ? kvh + (32 * kh + 4 * ((tr + ROT) & 7)) * KVT : WS_END; };
    bf16x8 kf[4], va[4], vb[4];
#define LOADT(u, so_) do { \
        va[u] = __builtin_bit_cast(bf16x8, __builtin_amdgcn_raw_buffer_load_b128(rs, vV[u], so_, SC1)); \
        vb[u] = __builtin_bit_cast(bf16x8, __builtin_amdgcn_raw_buffer_load_b128(rs, vV[u] + 640, so_, SC1)); \
        kf[u] = __builtin_bit_cast(bf16x8, __builtin_amdgcn_raw_buffer_load_b128(rs, vK[u], so_, SC1)); } while (0)
    unsigned soc = tsoff(0);
    u32x4 uv[2];
    if (!kh) {
#pragma unroll
        for (int c = 0; c < 2; ++c) uv[c] = __builtin_amdgcn_raw_buffer_load_b128(rs, WS_XT + (c0 + c) * 4096 + tid * 16, 0, SC1);
    }
    LOADT(0, soc); LOADT(1, soc); LOADT(2, soc);
    SBAR();
    vb[3] = __builtin_bit_cast(bf16x8, u32x4{0, 0, 0, 0});
    const bf16x8 qa = ldsr<bf16x8>(L_QX + hd * 2048 + lane * 16), qb = ldsr<bf16x8>(L_QX + hd * 2048 + 1024 + lane * 16);
    if (!kh) {
#pragma unroll
        for (int c = 0; c < 2; ++c) ldsw<u32x4>(L_UB + c * UBC + (32 + (tid >> 2)) * 80 + (tid & 3) * 16, uv[c]);
        asm volatile("s_waitcnt lgkmcnt(0)" ::: "memory");
        if (lane == 0) lds_add(L_CTL + C_U);
    }
    f32x16 nma, nmb, oa = zero16, ob = zero16, sa, sb = zero16;
    { const float ma = -rowmax(MFMA32(kf[0], qa, zero16)), mb = -rowmax(MFMA32(kf[0], qb, zero16));
#pragma unroll
      for (int i = 0; i < 16; ++i) { nma[i] = ma; nmb[i] = mb; } }
    sa = MFMA32(kf[0], qa, nma);
    STAMP(5);
#define EX(s, a, b) _Pragma("unroll") for (int i_ = (a); i_ < (b); ++i_) s[i_] = EXP2(s[i_]); SBAR()
    auto attn = [&](int t0, int t1) {
        for (int tr = t0; tr < t1; ++tr) {
            const unsigned son = tsoff(tr + 1);
            if (tr == ELOW && !kh) {
                __builtin_amdgcn_s_setprio(0);
                if (wid == 0) wait_flags((GAS u64*)(ws + WS_F2 + (oc * 32 + r) * 256), tag);
            }
#pragma unroll
            for (int u = 0; u < 4; ++u) {
                EX(sa, 0, 2); ob = SMF(vb[(u + 3) & 3], hi8(sb), ob); SBAR(); LOADT((u + 3) & 3, u ? son : soc); SBAR();
                EX(sa, 2, 5); sb = MFMA32(kf[u], qb, nmb); SBAR();
                EX(sa, 5, 11); oa = SMF(va[u], lo8(sa), oa); SBAR();
                EX(sa, 11, 16);
                EX(sb, 0, 2); oa = SMF(vb[u], hi8(sa), oa); SBAR();
                EX(sb, 2, 5); sa = MFMA32(kf[(u + 1) & 3], qa, nma); SBAR();
                EX(sb, 5, 11); ob = SMF(va[u], lo8(sb), ob); SBAR();
                EX(sb, 11, 16);
            }
            soc = son;
        }
    };
    const int ctrip = kh ? LTRIP : 0;
    attn(0, ctrip);
    STAMP(6);
    {
        __builtin_amdgcn_s_setprio(CPRIO);
        const int c = wid & 1, e = (wid >> 1) & 1, dlt = 16 * kh + 8 * e + 7;
        const int kap = (r + 1) & 3;
        unsigned ap = L_LO + c * LOC + kap * KCP + (2047 - r + kap + 8 * hh - 32 * dlt) * 2 - 2048;
        unsigned bp = L_UB + c * UBC + (32 + r - dlt) * 80 + hh * 16;
        const unsigned pA = L_PA + (c * 2 + e) * 8192 + lane * 16;
        f32x16 y0 = zero16, y1 = zero16;
        lds_wait(L_CTL + C_U, 4);
        if (kh) {
            lds_wait(L_CTL + C_E + 4 * (c * 2 + e), 1);
#pragma unroll
            for (int i = 0; i < 4; ++i) { const f32x4 p = ldsr<f32x4>(pA + i * 1024), q = ldsr<f32x4>(pA + 4096 + i * 1024);
#pragma unroll
                for (int k = 0; k < 4; ++k) { y0[4 * i + k] = p[k]; y1[4 * i + k] = q[k]; } }
        }
        struct Fr { u32x2 a0, a1, z0, z1; bf16x8 b0, b1; } f[2];
#define CA(i) (ap + 32 * (i))
#define CB(i) (bp + 80 * ((i) >> 1) + 32 * ((i) & 1))
#define CAT(x, y) __builtin_bit_cast(bf16x8, u32x4{x[0], x[1], y[0], y[1]})
        f[0].a0 = ldsr<u32x2>(CA(0) + 2048); f[0].a1 = ldsr<u32x2>(CA(0) + 2056); f[0].b0 = ldsr<bf16x8>(CB(0));
        f[0].b1 = ldsr<bf16x8>(CB(0) + 2560); f[0].z0 = ldsr<u32x2>(CA(0)); f[0].z1 = ldsr<u32x2>(CA(0) + 8);
        SBAR();
#pragma unroll
        for (int i = 0; i < 16; ++i) {
            Fr& q = f[i & 1]; Fr& n = f[(i + 1) & 1];
            y0 = MFMA32(CAT(q.a0, q.a1), q.b0, y0); SBAR();
            if (i < 15) { n.a0 = ldsr<u32x2>(CA(i + 1) + 2048); n.a1 = ldsr<u32x2>(CA(i + 1) + 2056); n.b0 = ldsr<bf16x8>(CB(i + 1)); } SBAR();
            y1 = MFMA32(CAT(q.a0, q.a1), q.b1, y1); SBAR();
            if (i < 15) { n.b1 = ldsr<bf16x8>(CB(i + 1) + 2560); n.z0 = ldsr<u32x2>(CA(i + 1)); n.z1 = ldsr<u32x2>(CA(i + 1) + 8); } SBAR();
            y1 = MFMA32(CAT(q.z0, q.z1), q.b0, y1); SBAR();
        }
        STAMP(27);
#pragma unroll
        for (int i = 0; i < 4; ++i) {
            if (!kh || e) ldsw<f32x4>(pA + i * 1024, f32x4{y0[4 * i], y0[4 * i + 1], y0[4 * i + 2], y0[4 * i + 3]});
            if (!kh || !e) ldsw<f32x4>(pA + 4096 + i * 1024, f32x4{y1[4 * i], y1[4 * i + 1], y1[4 * i + 2], y1[4 * i + 3]});
        }
        asm volatile("s_waitcnt lgkmcnt(0)" ::: "memory");
        if (lane == 0) lds_add(L_CTL + C_E + 4 * (c * 2 + e));
        if (kh) {
            lds_wait(L_CTL + C_E + 4 * (c * 2 + 1 - e), 2);
            f32x16 y = e ? y1 : y0;
            const unsigned pO = L_PA + (c * 2 + 1 - e) * 8192 + e * 4096 + lane * 16;
#pragma unroll
            for (int i = 0; i < 4; ++i) { const f32x4 p = ldsr<f32x4>(pO + i * 1024);
#pragma unroll
                for (int k = 0; k < 4; ++k) y[4 * i + k] += p[k]; }
            const float dsk = c ? bfhi(dk2) : bf2f(dk2 & 0xffff);
            u32x2 w[4];
#pragma unroll
            for (int i = 0; i < 4; ++i) {
                const u32x2 u = ldsr<u32x2>(L_UB + c * UBC + (32 + 32 * e + r) * 80 + i * 16 + hh * 8);
                const unsigned q0 = pk2(y[4 * i], y[4 * i + 1]), q1 = pk2(y[4 * i + 2], y[4 * i + 3]);
                const unsigned s0 = pk2(bf2f(u[0] & 0xffff) * dsk, bfhi(u[0]) * dsk), s1 = pk2(bf2f(u[1] & 0xffff) * dsk, bfhi(u[1]) * dsk);
                w[i] = u32x2{pk2(bf2f(q0 & 0xffff) + bf2f(s0 & 0xffff), bfhi(q0) + bfhi(s0)), pk2(bf2f(q1 & 0xffff) + bf2f(s1 & 0xffff), bfhi(q1) + bfhi(s1))};
            }
#pragma unroll
            for (int i = 0; i < 4; i += 2) {
                const auto s0 = __builtin_amdgcn_permlane32_swap(w[i][0], w[i + 1][0], false, false), s1 = __builtin_amdgcn_permlane32_swap(w[i][1], w[i + 1][1], false, false);
                __builtin_amdgcn_raw_buffer_store_b128(u32x4{s0[0], s1[0], s0[1], s1[1]}, rs, WS_BT + (c0 + c) * 4096 + (32 * e + r) * 64 + i * 16 + hh * 16, 0, SC1);
            }
            asm volatile("s_waitcnt vmcnt(0) lgkmcnt(0)" ::: "memory");
            if (lane == 0 && lds_add(L_CTL + C_CONV) == 3u) __hip_atomic_store((GAS u64*)(ws + WS_F2 + (hg * 32 + rt) * 256), tag, RLX_AGENT);
        } else {
            const unsigned vo = (128 * wid + (lane >> 3)) * 1024 + (((((lane & 7) >> 1) ^ g) * 2 + (lane & 1)) << 4);
            const char* s = (const char*)P.wo + oc * 128;
#pragma unroll
            for (int i = 0; i < 16; ++i) glds_s(s + i * 8192, vo, wid * 16384 + i * 1024);
        }
        __builtin_amdgcn_s_setprio(1);
    }
    STAMP(7);
    attn(ctrip, 8);
    ob = SMF(vb[3], hi8(sb), ob);
    STAMP(8);
    {
        const unsigned xw = L_XB + wid * XBW, xr = L_XB + (wid ^ 4) * XBW;
        const f32x16 os = kh ? oa : ob; f32x16 o = kh ? ob : oa;
        const float ms = kh ? nma[0] : nmb[0], mk = kh ? nmb[0] : nma[0];
        lds_wait(L_CTL + C_CONV, 4);
        ldsw<f32x4>(xw + lane * 16, f32x4{os[0], os[1], os[2], os[3]}); ldsw<f32x4>(xw + 1024 + lane * 16, f32x4{os[4], os[5], os[6], os[7]});
        ldsw<f32x2>(xw + 2048 + lane * 8, f32x2{os[8], ms});
        STAMP(30); BAR();
        const u32x4 bv = __builtin_amdgcn_raw_buffer_load_b128(rs, WS_BT + (64 * oc + (tid >> 3)) * 4096 + ort * 128 + (tid & 7) * 16, 0, SC1);
        SBAR();
        const f32x4 p0 = ldsr<f32x4>(xr + lane * 16), p1 = ldsr<f32x4>(xr + 1024 + lane * 16); const f32x2 p2 = ldsr<f32x2>(xr + 2048 + lane * 8);
        const float mn = fminf(mk, p2[1]), f0 = EXP2(mn - mk), f1 = EXP2(mn - p2[1]);
        float l = o[8] * f0 + p2[0] * f1;
        const bool safe = o[8] < 0x1p60f && p2[0] < 0x1p60f && l > 0x1p-30f;
#pragma unroll
        for (int i = 0; i < 4; ++i) { o[i] = o[i] * f0 + p0[i] * f1; o[4 + i] = o[4 + i] * f0 + p1[i] * f1; }
        if (!__all(safe)) {
            const bf16x8 q = kh ? qb : qa; float m = -INFINITY; o = zero16;
            for (int t = 0; t < 64; ++t) {
                const unsigned so = kvh + t * KVT;
                const bf16x8 k = __builtin_bit_cast(bf16x8, __builtin_amdgcn_raw_buffer_load_b128(rs, voffK, so, SC1));
                const bf16x8 v0 = __builtin_bit_cast(bf16x8, __builtin_amdgcn_raw_buffer_load_b128(rs, voffV, so, SC1));
                const bf16x8 v1 = __builtin_bit_cast(bf16x8, __builtin_amdgcn_raw_buffer_load_b128(rs, voffV + 640, so, SC1));
                f32x16 s = MFMA32(k, q, zero16);
                const float m2 = fmaxf(m, rowmax(s)), al = EXP2(m - m2); m = m2;
#pragma unroll
                for (int i = 0; i < 16; ++i) { s[i] = EXP2(s[i] - m); o[i] *= al; }
                o = SMF(v0, lo8(s), o); o = SMF(v1, hi8(s), o);
            }
            l = o[8];
        }
        const float rl = 1.0f / l;
        __builtin_amdgcn_raw_buffer_store_b128(u32x4{pk2(o[0] * rl, o[1] * rl), pk2(o[2] * rl, o[3] * rl), pk2(o[4] * rl, o[5] * rl), pk2(o[6] * rl, o[7] * rl)}, rs,
                                               WS_OF + ((rt * 4 + 2 * kh + (r >> 4)) * 16 + 2 * hg + (hd >> 1)) * 1024 + (2 * (hd & 1) + hh) * 256 + r16 * 16, 0, SC1);
        ldsw<u32x4>(L_BI + (tid >> 3) * BROW + (tid & 7) * 16, bv);
    }
    VBAR(0);
    STAMP(9);
    if (wid == 0) {
        if (lane == 0) __hip_atomic_store((GAS u64*)(ws + WS_F3 + (rt * 8 + hg) * 256), tag, RLX_AGENT);
    }
    const int rg = wid & 3;
    bf16x8 wf[8][4];
#pragma unroll
    for (int m = 0; m < 4; ++m) {
        const unsigned a = (256 * kh + 16 * (g >> 1) + 4 * (g & 1) + (r16 >> 2)) * 128 + ((m ^ (2 * (g & 1) + (r16 >> 3))) << 5) + (lane & 3) * 8;
#pragma unroll
        for (int ks = 0; ks < 8; ++ks) wf[ks][m] = trfrag(a + ks * 4096, a + ks * 4096 + 1024);
    }
    if (wid == 0) {
        wait_flags((GAS u64*)(ws + WS_F3 + (ort * 8 + (lane & 7)) * 256), tag);
        if (bid == 0 && lane == 0) __hip_atomic_store((GAS u64*)(ws + WS_NONCE), tag, RLX_AGENT);
    }
    BAR();
    STAMP(10);
    {
        const float gate = __uint_as_float(__builtin_amdgcn_raw_buffer_load_b32(rs, WS_G + (ort * 64 + 16 * rg + r16) * 4, 0, SC1));
        bf16x8 of[8];
#pragma unroll
        for (int ks = 0; ks < 8; ++ks) of[ks] = __builtin_bit_cast(bf16x8, __builtin_amdgcn_raw_buffer_load_b128(rs, WS_OF + ((ort * 4 + rg) * 16 + 8 * kh + ks) * 1024 + lane * 16, 0, SC1));
        SBAR();
        STAMP(28);
        f32x4 acc[4];
#pragma unroll
        for (int m = 0; m < 4; ++m) acc[m] = f32x4{0.f, 0.f, 0.f, 0.f};
#pragma unroll
        for (int ks = 0; ks < 8; ++ks)
#pragma unroll
            for (int m = 0; m < 4; ++m) acc[m] = MFMA16(wf[ks][m], of[ks], acc[m]);
        STAMP(29);
        const f32x4 k0 = kh ? acc[2] : acc[0], k1 = kh ? acc[3] : acc[1], s0 = kh ? acc[0] : acc[2], s1 = kh ? acc[1] : acc[3];
        ldsw<f32x4>(L_RED + wid * 2048 + lane * 16, s0); ldsw<f32x4>(L_RED + wid * 2048 + 1024 + lane * 16, s1);
        BAR();
        STAMP(11);
        const float omg = rbf(1.0f - gate);
#pragma unroll
        for (int e = 0; e < 2; ++e) {
            const f32x4 a = (e ? k1 : k0) + ldsr<f32x4>(L_RED + (wid ^ 4) * 2048 + e * 1024 + lane * 16);
            const u32x2 b = trread(L_BI + (16 * (2 * kh + e) + 4 * g + (r16 >> 2)) * BROW + rg * 32 + (lane & 3) * 8);
            const unsigned a0 = pk2(a[0], a[1]), a1 = pk2(a[2], a[3]);
            const unsigned ga0 = pk2(gate * bf2f(a0 & 0xffff), gate * bfhi(a0)), ga1 = pk2(gate * bf2f(a1 & 0xffff), gate * bfhi(a1));
            const unsigned gb0 = pk2(omg * bf2f(b[0] & 0xffff), omg * bfhi(b[0])), gb1 = pk2(omg * bf2f(b[1] & 0xffff), omg * bfhi(b[1]));
            *(u32x2*)(P.out + (ort * 64 + 16 * rg + r16) * 512 + 64 * oc + 16 * (2 * kh + e) + 4 * g) =
                u32x2{pk2(bf2f(ga0 & 0xffff) + bf2f(gb0 & 0xffff), bfhi(ga0) + bfhi(gb0)), pk2(bf2f(ga1 & 0xffff) + bf2f(gb1 & 0xffff), bfhi(ga1) + bfhi(gb1))};
        }
    }
    STAMP(12); STAMP_OUT();
}

__global__ void __launch_bounds__(64) probe_kernel(char* ws) {
    const u64 t0 = __builtin_amdgcn_s_memrealtime();
    for (int z = 0; z < 100000 && __builtin_amdgcn_s_memrealtime() < t0 + 3000; ++z) __builtin_amdgcn_s_sleep(2);
    if (threadIdx.x == 0) __hip_atomic_store((GAS u64*)(ws + WS_STAMP), (u64)__builtin_amdgcn_s_memrealtime(), RLX_AGENT);
}
extern "C" void kernel_launch(void* const* d_in, const int* in_sizes, int n_in, void* d_out, int out_size, void* d_ws, size_t ws_size, hipStream_t stream) {
    Params p;
    p.x = (const u16*)d_in[0]; p.wqkv = (const u16*)d_in[1]; p.wo = (const u16*)d_in[2];
    p.are = (const u16*)d_in[4]; p.aim = (const u16*)d_in[5]; p.bre = (const u16*)d_in[6]; p.bim = (const u16*)d_in[7];
    p.cre = (const u16*)d_in[8]; p.cim = (const u16*)d_in[9]; p.logdt = (const u16*)d_in[10]; p.dskip = (const u16*)d_in[11]; p.gw = (const u16*)d_in[12];
    p.out = (u16*)d_out; p.ws = (char*)d_ws;
    (void)hipFuncSetAttribute((const void*)fused_kernel, hipFuncAttributeMaxDynamicSharedMemorySize, LDS_BYTES);
    probe_kernel<<<1, 64, 0, stream>>>((char*)d_ws);
    fused_kernel<<<256, 512, LDS_BYTES, stream>>>(p);
}
```

```cpp
#include <hip/hip_runtime.h>
#include <hip/hip_bf16.h>
#include <stdint.h>

typedef float f32x2 __attribute__((ext_vector_type(2)));
typedef float f32x4 __attribute__((ext_vector_type(4)));
typedef float f32x8 __attribute__((ext_vector_type(8)));
typedef float f32x16 __attribute__((ext_vector_type(16)));
typedef __bf16 bf16x2 __attribute__((ext_vector_type(2)));
typedef __bf16 bf16x8 __attribute__((ext_vector_type(8)));
typedef __bf16 bf16x16 __attribute__((ext_vector_type(16)));
typedef short s16x4 __attribute__((ext_vector_type(4)));
typedef unsigned u32x2 __attribute__((ext_vector_type(2)));
typedef unsigned u32x4 __attribute__((ext_vector_type(4)));
typedef unsigned short u16;
typedef unsigned long long u64;
#define LAS __attribute__((address_space(3)))
#define GAS __attribute__((address_space(1)))
#define CAS __attribute__((address_space(4)))
#define RLX_AGENT __ATOMIC_RELAXED, __HIP_MEMORY_SCOPE_AGENT
#define SC1 16
#define NA 1
#define LTRIP 4
#define ELOW 7
#define ROT rt
#define CPRIO 3

constexpr unsigned WS_NONCE = 0, WS_F1 = 4096, WS_F2 = WS_F1 + 65536, WS_F3 = WS_F2 + 65536;
constexpr unsigned WS_G = WS_F3 + 65536;
constexpr unsigned WS_KV = 262144, KVT = 2304;
constexpr unsigned WS_XT = WS_KV + 32 * 64 * KVT;
constexpr unsigned WS_BT = WS_XT + 2097152;
constexpr unsigned WS_OF = WS_BT + 2097152;
constexpr unsigned WS_END = WS_OF + 2097152;
constexpr unsigned WS_STAMP = 16u << 20;
constexpr unsigned SLOT = 24576, TROW = 528;
constexpr unsigned slot_at(int c) { return ((c + 4) % 6) * SLOT; }
constexpr unsigned L_LO = 73728, LOC = 32 * TROW;
constexpr unsigned L_HI = 107520, HIC = 27 * TROW;
constexpr unsigned L_PAR = 147456;
constexpr unsigned L_CTL = 163712, LDS_BYTES = 163840;
constexpr unsigned L_QX = 65536;
constexpr unsigned KCP = 4224;
constexpr unsigned L_UB = 147456, UBC = 7680;
constexpr unsigned L_PA = 107520;
constexpr unsigned L_XB = 73728, XBW = 2560;
constexpr unsigned L_BI = 107520, BROW = 144, L_RED = 122880;
enum { C_KV = 0, C_WQ = 4, C_U = 8, C_CONV = 12, C_TAB = 16, C_R7 = 20, C_GO = 24, C_E = 32 };
constexpr float QSCALE = 0.25f * 1.4426950408889634f;
constexpr int SMF_IDX = (int)0xDDDDDDDD;

__device__ __forceinline__ float bf2f(unsigned v) { return __uint_as_float(v << 16); }
__device__ __forceinline__ float bfhi(unsigned v) { return __uint_as_float(v & 0xffff0000u); }
__device__ __forceinline__ unsigned pk2(float lo, float hi) { bf16x2 v = {(__bf16)lo, (__bf16)hi}; return __builtin_bit_cast(unsigned, v); }
__device__ __forceinline__ float rbf(float v) { return bf2f(__builtin_bit_cast(u16, (__bf16)v)); }
__device__ __forceinline__ u32x2 trread(unsigned a) { return __builtin_bit_cast(u32x2, __builtin_amdgcn_ds_read_tr16_b64_v4i16((LAS s16x4*)a)); }
__device__ __forceinline__ bf16x8 trfrag(unsigned a0, unsigned a1) { const u32x2 a = trread(a0), b = trread(a1); return __builtin_bit_cast(bf16x8, u32x4{a[0], a[1], b[0], b[1]}); }
template <typename T> __device__ __forceinline__ T ldsr(unsigned a) { return *(LAS const T*)a; }
template <typename T> __device__ __forceinline__ void ldsw(unsigned a, T v) { *(LAS T*)a = v; }
__device__ __forceinline__ void glds_s(const void* sbase, unsigned voff, unsigned lds) {
    asm volatile("s_mov_b32 m0, %2\n\ts_nop 0\n\tglobal_load_lds_dwordx4 %0, %1" :: "v"(voff), "s"(sbase), "s"(lds) : "memory");
}
__device__ __forceinline__ void glds_v(const void* vaddr, unsigned lds) {
    asm volatile("s_mov_b32 m0, %1\n\ts_nop 0\n\tglobal_load_lds_dwordx4 %0, off" :: "v"(vaddr), "s"(lds) : "memory");
}
#define BAR() asm volatile("s_waitcnt lgkmcnt(0)\n\ts_barrier" ::: "memory")
#define VBAR(n) asm volatile("s_waitcnt vmcnt(" #n ") lgkmcnt(0)\n\ts_barrier" ::: "memory")
#define SBAR() __builtin_amdgcn_sched_barrier(0)
#define MFMA32(a, b, c) __builtin_amdgcn_mfma_f32_32x32x16_bf16(a, b, c, 0, 0, 0)
#define MFMA16(a, b, c) __builtin_amdgcn_mfma_f32_16x16x32_bf16(a, b, c, 0, 0, 0)
#define SMF(a, p, o) __builtin_amdgcn_smfmac_f32_32x32x32_bf16(a, __builtin_bit_cast(bf16x16, p), o, SMF_IDX, 0, 0)
#define EXP2(v) __builtin_amdgcn_exp2f(v)
#define STAMP(i)
#define STAMP_DECL
#define STAMP_OUT()

struct C2 { float r0, i0, r1, i1; };
__device__ __forceinline__ C2 cmul2(const C2& a, const C2& b) {
    C2 d;
    asm("v_mul_f32 %0, %4, %8\n\tv_mul_f32 %1, %5, %8\n\tv_mul_f32 %2, %6, %10\n\tv_mul_f32 %3, %7, %10\n\t"
        "v_fma_f32 %0, -%5, %9, %0\n\tv_fma_f32 %1, %4, %9, %1\n\tv_fma_f32 %2, -%7, %11, %2\n\tv_fma_f32 %3, %6, %11, %3"
        : "=&v"(d.r0), "=&v"(d.i0), "=&v"(d.r1), "=&v"(d.i1) : "v"(a.r0), "v"(a.i0), "v"(a.r1), "v"(a.i1), "v"(b.r0), "v"(b.i0), "v"(b.r1), "v"(b.i1));
    return d;
}
__device__ __forceinline__ u32x2 cpack(const C2& a) { return u32x2{pk2(a.r0, a.i0), pk2(a.r1, a.i1)}; }
__device__ __forceinline__ f32x2 cexpm(float al2, float rev, float mult) {
    const float e = EXP2(al2 * mult), f = __builtin_amdgcn_fractf(rev * mult);
    return f32x2{e * __builtin_amdgcn_cosf(f), e * __builtin_amdgcn_sinf(f)};
}
__device__ __forceinline__ C2 cexp2(float l0, float v0, float l1, float v1, float mult) {
    const f32x2 a = cexpm(l0, v0, mult), b = cexpm(l1, v1, mult); return C2{a[0], a[1], b[0], b[1]};
}
__device__ __forceinline__ void wait_flags(GAS u64* p, u64 tag) {
    for (int spins = 0; spins < (1 << 18); ++spins) {
        if (__all(__hip_atomic_load(p, RLX_AGENT) == tag)) break;
        __builtin_amdgcn_s_sleep(2);
    }
}
__device__ __forceinline__ f32x8 lo8(const f32x16& s) { return __builtin_shufflevector(s, s, 0, 1, 2, 3, 4, 5, 6, 7); }
__device__ __forceinline__ f32x8 hi8(const f32x16& s) { return __builtin_shufflevector(s, s, 8, 9, 10, 11, 12, 13, 14, 15); }
__device__ __forceinline__ float rowmax(const f32x16& s) {
    float m = fmaxf(fmaxf(s[0], s[1]), s[2]);
#pragma unroll
    for (int i = 3; i < 15; i += 2) m = fmaxf(fmaxf(m, s[i]), s[i + 1]);
    m = fmaxf(m, s[15]);
    const auto r = __builtin_amdgcn_permlane32_swap(__float_as_uint(m), __float_as_uint(m), false, false);
    return fmaxf(__uint_as_float(r[0]), __uint_as_float(r[1]));
}

struct Params {
    const u16 *x, *wqkv, *wo, *are, *aim, *bre, *bim, *cre, *cim, *logdt, *dskip, *gw;
    u16* out; char* ws;
};
__device__ __forceinline__ unsigned lds_add(unsigned a) { return __hip_atomic_fetch_add((LAS unsigned*)a, 1u, __ATOMIC_RELAXED, __HIP_MEMORY_SCOPE_WORKGROUP); }
__device__ __forceinline__ void lds_wait(unsigned a, unsigned n) {
    for (int sp = 0; sp < (1 << 22) && *(volatile LAS unsigned*)a < n; ++sp) __builtin_amdgcn_s_sleep(1);
    asm volatile("" ::: "memory");
}
__global__ void __launch_bounds__(512, 2) fused_kernel(Params P) {
    char* const ws = P.ws;
    const int tid = threadIdx.x, lane = tid & 63, wid = __builtin_amdgcn_readfirstlane(tid >> 6), bid = blockIdx.x;
    const int xcd = bid & 7, jb = bid >> 3;
    const int hg = (xcd % (8 / NA)) * NA + (jb % NA), rt = (xcd / (8 / NA)) * (32 / NA) + jb / NA;
    const int ort = 4 * xcd + (jb >> 3), oc = jb & 7;
    const int c0 = 64 * hg + 2 * rt;
    const int r = lane & 31, hh = lane >> 5, g = lane >> 4, r16 = lane & 15;
    STAMP_DECL; STAMP(0);
    asm volatile("" :: "s"(P.x), "s"(P.wqkv), "s"(P.are), "s"(P.aim), "s"(P.bre), "s"(P.bim), "s"(P.cre), "s"(P.cim), "s"(P.logdt), "s"(P.dskip), "s"(P.gw), "s"(P.ws), "s"(P.wo), "s"(P.out));
    const int tt = wid & 1, pp = (wid >> 1) & 1;
    f32x16 zero16;
#pragma unroll
    for (int i = 0; i < 16; ++i) zero16[i] = 0.f;
    const __amdgpu_buffer_rsrc_t rs = __builtin_amdgcn_make_buffer_rsrc(ws, 0, WS_END, 0x00020000);
    u64 tag;
    unsigned dk2;

    if (__builtin_expect(wid < 4, 1)) {
        const unsigned vx0 = (16 * wid + (lane >> 3)) * 1024 + (((lane & 7) ^ g) << 4), vx1 = vx0 + 8192 + ((((lane & 7) ^ g ^ 4) - ((lane & 7) ^ g)) << 4);
        const unsigned vw = (16 * wid + (lane >> 3)) * 3072 + (((((lane & 7) >> 1) ^ ((g & 1) << 1)) * 2 + (lane & 1)) << 4);
        const unsigned vw1 = vw + 24576;
        const char* xb = (const char*)P.x + rt * 65536;
        const char* wb = (const char*)P.wqkv + hg * 128;
        auto dmaW = [&](int m, int cc, unsigned dst) { const char* s = wb + m * 1024 + ((cc + hg) & 7) * 196608;
            glds_s(s, vw, dst + wid * 2048); glds_s(s, vw1, dst + wid * 2048 + 1024); };
        auto dmaC = [&](int cc) { const unsigned sl = slot_at(cc); const char* s = xb + ((cc + hg) & 7) * 128;
            glds_s(s, vx0, sl + wid * 2048); glds_s(s, vx1, sl + wid * 2048 + 1024); dmaW(1, cc, sl + 8192); dmaW(2, cc, sl + 16384); };
        dmaC(0);
        {
            const int a = wid < 3 ? wid : 2;
            const u16* s0 = a == 0 ? P.are : a == 1 ? P.bre : P.cre;
            const u16* s1 = a == 0 ? P.aim : a == 1 ? P.bim : P.cim;
            glds_v((const char*)(hh ? s1 : s0) + c0 * 256 + r * 16, L_PAR + a * 1024);
        }
        STAMP(13);
        dmaC(1); dmaC(2);
        STAMP(14);
        const unsigned ld2 = ((CAS const unsigned*)P.logdt)[c0 >> 1];
        dk2 = ((CAS const unsigned*)P.dskip)[c0 >> 1];
        const float dt = rbf(__expf(tt ? bfhi(ld2) : bf2f(ld2 & 0xffff)));
        u32x2 lv[16], hv[14];
#define PINV(v, n) _Pragma("unroll") for (int i_ = 0; i_ < (n); ++i_) asm volatile("" : "+v"(v[i_]))
        VBAR(12); STAMP(1); dmaC(3);
        const unsigned pb = L_PAR + tt * 256 + lane * 4;
        const unsigned pr = ldsr<unsigned>(pb), pi = ldsr<unsigned>(pb + 512), br = ldsr<unsigned>(pb + 1024), bi = ldsr<unsigned>(pb + 1536), cr = ldsr<unsigned>(pb + 2048), ci = ldsr<unsigned>(pb + 2560);
        VBAR(12); STAMP(16); dmaC(4);
        const float l0 = dt * bf2f(pr & 0xffff) * 1.4426950408889634f, l1 = dt * bfhi(pr) * 1.4426950408889634f;
        const float v0 = dt * bf2f(pi & 0xffff) * 0.15915494309189535f, v1 = dt * bfhi(pi) * 0.15915494309189535f;
        C2 e = cexp2(l0, v0, l1, v1, 8.f), a = cexp2(l0, v0, l1, v1, 128.f * pp);
#define LO(j0, j1) _Pragma("unroll") for (int j = (j0); j < (j1); ++j) { lv[j] = cpack(a); if (j < 15) a = cmul2(a, e); } PINV(lv, 16); SBAR()
        SBAR();
        VBAR(12); STAMP(17); dmaC(5); LO(0, 4);
        VBAR(12); STAMP(18); dmaC(6); LO(4, 8);
        VBAR(12); STAMP(19); dmaC(7); LO(8, 12);
        VBAR(12); STAMP(20); dmaW(0, 0, 0); dmaW(0, 1, 8192); dmaW(0, 2, 16384); LO(12, 16);
        VBAR(12); STAMP(21); dmaW(0, 3, 24576); dmaW(0, 4, 32768); dmaW(0, 5, 40960);
        a = cmul2(C2{bf2f(cr & 0xffff), -bf2f(ci & 0xffff), bfhi(cr), -bfhi(ci)}, C2{dt * bf2f(br & 0xffff), -dt * bf2f(bi & 0xffff), dt * bfhi(br), -dt * bfhi(bi)});
        const C2 gg = cexp2(l0, -v0, l1, -v1, 256.f);
        e = cexp2(l0, -v0, l1, -v1, pp ? 4.f : 1.f);
        SBAR();
        VBAR(12); STAMP(22); dmaW(0, 6, 49152); dmaW(0, 7, 57344);
        tag = __hip_atomic_load((GAS u64*)(ws + WS_NONCE), RLX_AGENT) + 1;
        hv[13] = u32x2{0, 0};
        if (pp == 0) {
#pragma unroll
            for (int j = 0; j < 9; ++j) { if (j < 8) hv[j] = cpack(a); if (!(j & 1)) hv[8 + (j >> 1)] = cpack(cmul2(a, gg)); if (j < 8) a = cmul2(a, e); }
            PINV(hv, 13);
        } else {
            const C2 f = cmul2(e, e);
#pragma unroll
            for (int k = 1; k < 8; ++k) { a = cmul2(a, gg); if (k >= 2) { const int row = k < 4 ? 3 * (k - 2) : 6 + 2 * (k - 4); hv[row] = cpack(a);
                if (k < 4) hv[row + 1] = cpack(cmul2(a, e)); hv[row + (k < 4 ? 2 : 1)] = cpack(cmul2(a, f)); } }
            PINV(hv, 14);
        }
        STAMP(2);
        asm volatile("s_waitcnt vmcnt(0)" ::: "memory");
        if (lane == 0) lds_add(L_CTL + C_WQ);
        lds_wait(L_CTL + C_R7, 4);
        STAMP(3);
        const unsigned tl = L_LO + tt * LOC + 16 * pp * TROW + lane * 8, th = L_HI + tt * HIC + 13 * pp * TROW + lane * 8;
#pragma unroll
        for (int i = 0; i < 16; ++i) { ldsw<u32x2>(tl + i * TROW, lv[i]); if (i < 13 || (i == 13 && pp)) ldsw<u32x2>(th + i * TROW, hv[i]); }
        asm volatile("s_waitcnt lgkmcnt(0)" ::: "memory");
        if (lane == 0) lds_add(L_CTL + C_TAB);
        STAMP(23);
        if (wid == 3) {
            wait_flags((GAS u64*)(ws + WS_F1 + (hg * 32 + r) * 256), tag);
            if (lane == 0) lds_add(L_CTL + C_GO);
        } else if (wid == 2) {
            lds_wait(L_CTL + C_TAB, 4);
#pragma unroll
            for (int i = 0; i < 5; ++i) ldsw<u32x4>(L_UB + hh * UBC + (r + 32 * i) * 16, u32x4{0, 0, 0, 0});
        } else {
            const int c = wid;
            const u64 mp = r < 8 ? 0x0706050403020100ull : r < 16 ? 0x121110100b0a0908ull : r < 24 ? 0x000000120f0e0d0cull : 0x1a19181716151413ull;
            const unsigned aa = L_HI + c * HIC + (unsigned)((mp >> ((r & 7) * 8)) & 0xff) * TROW + hh * 16, ba = L_LO + c * LOC + r * TROW + hh * 16;
            lds_wait(L_CTL + C_TAB, 4);
            bf16x8 ta[16], tb[16];
#pragma unroll
            for (int s = 0; s < 4; ++s) { ta[s] = ldsr<bf16x8>(aa + s * 32); tb[s] = ldsr<bf16x8>(ba + s * 32); }
            SBAR();
            f32x16 k = zero16;
#pragma unroll
            for (int s = 0; s < 16; ++s) { k = MFMA32(ta[s], tb[s], k); SBAR(); if (s < 12) { ta[s + 4] = ldsr<bf16x8>(aa + s * 32 + 128); tb[s + 4] = ldsr<bf16x8>(ba + s * 32 + 128); } SBAR(); }
            STAMP(15);
            asm volatile("" ::: "memory");
            const unsigned K0 = L_LO + c * LOC, R = K0 + (2040 - 8 * r) * 2;
            ldsw<u32x2>(R + (1 - hh) * 8, u32x2{pk2(k[3], k[2]), pk2(k[1], k[0])});
            ldsw<u32x4>(R - (32 + 64 * hh) * 16, u32x4{pk2(k[8], k[7]), pk2(k[6], k[6]), pk2(k[6], k[5]), pk2(k[4], k[4])});
            if (hh == 0) ldsw<u32x4>(R - 64 * 16, u32x4{pk2(k[11], k[11]), pk2(k[10], k[10]), pk2(k[10], k[9]), pk2(k[9], k[9])});
#pragma unroll
            for (int e_ = 0; e_ < 2; ++e_) { const float a_ = k[12 + 2 * e_], b_ = k[13 + 2 * e_]; const unsigned aa2 = pk2(a_, a_);
                ldsw<u32x4>(R - (128 + 32 * e_ + 64 * hh) * 16, u32x4{pk2(b_, b_), pk2(b_, (r & 1) ? b_ : a_), aa2, aa2}); }
            if (lane < 8) ldsw<u32x4>(K0 + 4096 + lane * 16, u32x4{0, 0, 0, 0});
            asm volatile("" ::: "memory");
            u32x4 cu[5]; u32x2 pv[5];
#pragma unroll
            for (int ps = 0; ps < 5; ++ps) { const unsigned a_ = K0 + (ps < 4 ? 64 * ps + lane : 256 + (lane & 7)) * 16; cu[ps] = ldsr<u32x4>(a_); pv[ps] = ldsr<u32x2>(a_ - 8); }
#pragma unroll
            for (int ps = 0; ps < 5; ++ps) {
                const unsigned am = __builtin_amdgcn_alignbit(pv[ps][1], pv[ps][0], 16), a0 = __builtin_amdgcn_alignbit(cu[ps][0], pv[ps][1], 16), a1 = __builtin_amdgcn_alignbit(cu[ps][1], cu[ps][0], 16),
                               a2 = __builtin_amdgcn_alignbit(cu[ps][2], cu[ps][1], 16), a3 = __builtin_amdgcn_alignbit(cu[ps][3], cu[ps][2], 16);
                const unsigned W = K0 + KCP + (ps < 4 ? 64 * ps + lane : 256 + (lane & 7)) * 16;
                ldsw<u32x4>(W, u32x4{a0, a1, a2, a3}); ldsw<u32x4>(W + KCP, u32x4{pv[ps][1], cu[ps][0], cu[ps][1], cu[ps][2]}); ldsw<u32x4>(W + 2 * KCP, u32x4{am, a0, a1, a2});
            }
        }
    } else {
        if (wid == 4 && lane < 16) ldsw<unsigned>(L_CTL + lane * 4, 0u);
        __builtin_amdgcn_s_setprio(2);
        const int w4 = wid - 4;
        tag = __hip_atomic_load((GAS u64*)(ws + WS_NONCE), RLX_AGENT) + 1;
        {
            const int grow = rt * 64 + 8 * hg + 2 * w4;
            const u32x4 gv = *(const u32x4*)(P.gw + lane * 8), x0 = *(const u32x4*)(P.x + grow * 512 + lane * 8), x1 = *(const u32x4*)(P.x + grow * 512 + 512 + lane * 8);
            float s0 = 0.f, s1 = 0.f;
#pragma unroll
            for (int j = 0; j < 4; ++j) { const unsigned gj = gv[j], aj = x0[j], bj = x1[j];
                s0 = __builtin_amdgcn_fdot2_f32_bf16(__builtin_bit_cast(bf16x2, aj), __builtin_bit_cast(bf16x2, gj), s0, false);
                s1 = __builtin_amdgcn_fdot2_f32_bf16(__builtin_bit_cast(bf16x2, bj), __builtin_bit_cast(bf16x2, gj), s1, false);
            }
            const auto sa = __builtin_amdgcn_permlane32_swap(__float_as_uint(s0), __float_as_uint(s1), false, false);
            const unsigned t0 = __float_as_uint(__uint_as_float(sa[0]) + __uint_as_float(sa[1]));
            const auto sc = __builtin_amdgcn_permlane16_swap(t0, t0, false, false);
            float t = __uint_as_float(sc[0]) + __uint_as_float(sc[1]);
            t += __uint_as_float(__builtin_amdgcn_update_dpp(0u, __float_as_uint(t), 0x128, 0xf, 0xf, false));
            t += __uint_as_float(__builtin_amdgcn_update_dpp(0u, __float_as_uint(t), 0x124, 0xf, 0xf, false));
            t += __uint_as_float(__builtin_amdgcn_update_dpp(0u, __float_as_uint(t), 0x122, 0xf, 0xf, false));
            t += __uint_as_float(__builtin_amdgcn_update_dpp(0u, __float_as_uint(t), 0x121, 0xf, 0xf, false));
            const float gate = rbf(1.0f / (1.0f + __expf(-rbf(t))));
            if (r == 0) __builtin_amdgcn_raw_buffer_store_b32(__float_as_uint(gate), rs, WS_G + (grow + hh) * 4, 0, SC1);
        }
        dk2 = ((CAS const unsigned*)P.dskip)[c0 >> 1];
        unsigned xa[2][4], wk[2];
#pragma unroll
        for (int ks = 0; ks < 4; ++ks) { xa[0][ks] = (32 * tt + r) * 128 + (((2 * ks + hh) ^ ((r >> 1) & 7)) << 4); xa[1][ks] = xa[0][ks] + 3 * SLOT; asm volatile("" : "+v"(xa[1][ks])); }
        const unsigned wa = (8 * hh + (r16 >> 2)) * 128 + (((2 * pp + (g & 1)) ^ (r16 >> 3 << 1)) << 5) + (lane & 3) * 8;
        wk[0] = wa + 8192; wk[1] = wa + 8192 + 3 * SLOT; asm volatile("" : "+v"(wk[0]), "+v"(wk[1]));
        f32x16 ak = zero16, av = zero16;
        bf16x8 xq[32];
        u32x2 fk[4][2], fv[4][2];
#define SH(cc) (slot_at(cc) >= 3 * SLOT)
#define SO(cc) (slot_at(cc) % (3 * SLOT))
#define RX(cc, ks) xq[4 * (cc) + (ks)] = ldsr<bf16x8>(xa[SH(cc)][ks] + SO(cc))
#define RK(cc, ks, t) fk[ks][t] = trread(wk[SH(cc)] + SO(cc) + (ks) * 2048 + (t) * 512)
#define RV(cc, ks, t) fv[ks][t] = trread(wk[SH(cc)] + SO(cc) + 8192 + (ks) * 2048 + (t) * 512)
#define FR(f, ks) __builtin_bit_cast(bf16x8, u32x4{f[ks][0][0], f[ks][0][1], f[ks][1][0], f[ks][1][1]})
#define MK(cc, ks) ak = MFMA32(FR(fk, ks), xq[4 * (cc) + (ks)], ak); SBAR()
#define MV(cc, ks) av = MFMA32(xq[4 * (cc) + (ks)], FR(fv, ks), av); SBAR()
#pragma unroll
        for (int cc = 0; cc < 8; ++cc) {
            STAMP(14 + cc); BAR();
            if (cc) { MK(cc - 1, 2); } RX(cc, 0); RK(cc, 0, 0); RK(cc, 0, 1); SBAR();
            if (cc) { MV(cc - 1, 2); } RV(cc, 0, 0); RV(cc, 0, 1); RX(cc, 1); RK(cc, 1, 0); SBAR();
            if (cc) { MK(cc - 1, 3); } RK(cc, 1, 1); RV(cc, 1, 0); RV(cc, 1, 1); SBAR();
            if (cc) { MV(cc - 1, 3); } RX(cc, 2); RK(cc, 2, 0); RK(cc, 2, 1); RV(cc, 2, 0); SBAR();
            MK(cc, 0); RV(cc, 2, 1); RX(cc, 3); RK(cc, 3, 0); SBAR();
            MV(cc, 0); RK(cc, 3, 1); RV(cc, 3, 0); RV(cc, 3, 1); SBAR();
            if (cc == 0) {
                u32x2 t[4];
#pragma unroll
                for (int n = 0; n < 4; ++n) { const int row = 16 * g + 4 * n + (r16 >> 2);
                    t[n] = trread(slot_at(0) + row * 128 + (((2 * w4 + ((lane & 3) >> 1)) ^ ((row >> 1) & 7)) << 4) + (lane & 1) * 8); }
                MK(cc, 1); MV(cc, 1);
                const unsigned o = WS_XT + (64 * hg + 16 * w4 + r16) * 4096 + rt * 128 + g * 32;
                __builtin_amdgcn_raw_buffer_store_b128(u32x4{t[0][0], t[0][1], t[1][0], t[1][1]}, rs, o, 0, SC1);
                __builtin_amdgcn_raw_buffer_store_b128(u32x4{t[2][0], t[2][1], t[3][0], t[3][1]}, rs, o + 16, 0, SC1);
                SBAR();
            } else { MK(cc, 1); MV(cc, 1); }
        }
        MK(7, 2); MV(7, 2); MK(7, 3); MV(7, 3);
        STAMP(1);
        {
            const unsigned tb = WS_KV + ((4 * hg + 2 * pp) * 64 + 2 * rt + tt) * KVT;
            const int rp = (r & 19) | ((r & 4) << 1) | ((r & 8) >> 1);
            const unsigned vb = tb + (r >> 4) * (64 * KVT) + 1024;
#pragma unroll
            for (int e = 0; e < 2; ++e) {
                __builtin_amdgcn_raw_buffer_store_b128(u32x4{pk2(ak[8 * e], ak[8 * e + 1]), pk2(ak[8 * e + 2], ak[8 * e + 3]), pk2(ak[8 * e + 4], ak[8 * e + 5]), pk2(ak[8 * e + 6], ak[8 * e + 7])},
                                                       rs, tb + e * (64 * KVT) + hh * 512 + rp * 16, 0, SC1);
                __builtin_amdgcn_raw_buffer_store_b128(u32x4{pk2(av[8 * e], av[8 * e + 1]), pk2(av[8 * e + 2], av[8 * e + 3]), pk2(av[8 * e + 4], av[8 * e + 5]), pk2(av[8 * e + 6], av[8 * e + 7])},
                                                       rs, vb + e * 640 + hh * 256 + r16 * 16, 0, SC1);
            }
            if (r16 == 0) __builtin_amdgcn_raw_buffer_store_b128(u32x4{0x3f803f80u, 0x3f803f80u, 0x3f803f80u, 0x3f803f80u}, rs, vb + hh * 640 + 512, 0, SC1);
        }
        if (lane == 0) lds_add(L_CTL + C_R7);
        STAMP(2);
        asm volatile("s_waitcnt vmcnt(0) lgkmcnt(0)" ::: "memory");
        if (lane == 0 && lds_add(L_CTL + C_KV) == 3u) __hip_atomic_store((GAS u64*)(ws + WS_F1 + (hg * 32 + rt) * 256), tag, RLX_AGENT);
        STAMP(3);
        lds_wait(L_CTL + C_WQ, 4);
        STAMP(23);
        f32x16 aq = zero16;
        bf16x8 wq[8];
#pragma unroll
        for (int ks = 0; ks < 8; ++ks) wq[ks] = trfrag(ks * 2048 + wa, ks * 2048 + wa + 512);
        SBAR();
#pragma unroll
        for (int i = 0; i < 32; ++i) {
            aq = MFMA32(wq[i & 7], xq[i], aq); SBAR();
            if (i < 24) wq[i & 7] = trfrag((i + 8) * 2048 + wa, (i + 8) * 2048 + wa + 512);
            SBAR();
        }
#pragma unroll
        for (int e_ = 0; e_ < 2; ++e_)
            ldsw<u32x4>(L_QX + ((2 * pp + e_) * 2 + tt) * 1024 + lane * 16,
                        u32x4{pk2(aq[8 * e_] * QSCALE, aq[8 * e_ + 1] * QSCALE), pk2(aq[8 * e_ + 2] * QSCALE, aq[8 * e_ + 3] * QSCALE),
                              pk2(aq[8 * e_ + 4] * QSCALE, aq[8 * e_ + 5] * QSCALE), pk2(aq[8 * e_ + 6] * QSCALE, aq[8 * e_ + 7] * QSCALE)});
        asm volatile("s_waitcnt lgkmcnt(0)" ::: "memory");
        if (lane == 0) lds_add(L_CTL + C_GO);
    }
    STAMP(24);
    lds_wait(L_CTL + C_GO, 5);
    STAMP(4);
    const int hd = wid & 3, kh = wid >> 2;
    if (kh) __builtin_amdgcn_s_setprio(2); else __builtin_amdgcn_s_setprio(3);
    const unsigned voffK = lane * 16, voffV = r < 16 ? 1024 + hh * 256 + r * 16 : (r == 16 || r == 20) ? 1536 : 0x4000000;
    const unsigned kvh = WS_KV + (4 * hg + hd) * 64 * KVT;
    unsigned vK[4], vV[4];
#pragma unroll
    for (int u = 0; u < 4; ++u) { vK[u] = voffK + u * KVT; vV[u] = voffV + u * KVT; asm volatile("" : "+v"(vK[u]), "+v"(vV[u])); }
    auto tsoff = [&](int tr) -> unsigned { return tr < 8 ? kvh + (32 * kh + 4 * ((tr + ROT) & 7)) * KVT : WS_END; };
    bf16x8 kf[4], va[4], vb[4];
#define LOADT(u, so_) do { \
        va[u] = __builtin_bit_cast(bf16x8, __builtin_amdgcn_raw_buffer_load_b128(rs, vV[u], so_, SC1)); \
        vb[u] = __builtin_bit_cast(bf16x8, __builtin_amdgcn_raw_buffer_load_b128(rs, vV[u] + 640, so_, SC1)); \
        kf[u] = __builtin_bit_cast(bf16x8, __builtin_amdgcn_raw_buffer_load_b128(rs, vK[u], so_, SC1)); } while (0)
    unsigned soc = tsoff(0);
    u32x4 uv[2];
    if (!kh) {
#pragma unroll
        for (int c = 0; c < 2; ++c) uv[c] = __builtin_amdgcn_raw_buffer_load_b128(rs, WS_XT + (c0 + c) * 4096 + tid * 16, 0, SC1);
    }
    LOADT(0, soc); LOADT(1, soc); LOADT(2, soc);
    SBAR();
    vb[3] = __builtin_bit_cast(bf16x8, u32x4{0, 0, 0, 0});
    const bf16x8 qa = ldsr<bf16x8>(L_QX + hd * 2048 + lane * 16), qb = ldsr<bf16x8>(L_QX + hd * 2048 + 1024 + lane * 16);
    if (!kh) {
#pragma unroll
        for (int c = 0; c < 2; ++c) ldsw<u32x4>(L_UB + c * UBC + (32 + (tid >> 2)) * 80 + (tid & 3) * 16, uv[c]);
        asm volatile("s_waitcnt lgkmcnt(0)" ::: "memory");
        if (lane == 0) lds_add(L_CTL + C_U);
    }
    f32x16 nma, nmb, oa = zero16, ob = zero16, sa, sb = zero16;
    { const float ma = -rowmax(MFMA32(kf[0], qa, zero16)), mb = -rowmax(MFMA32(kf[0], qb, zero16));
#pragma unroll
      for (int i = 0; i < 16; ++i) { nma[i] = ma; nmb[i] = mb; } }
    sa = MFMA32(kf[0], qa, nma);
    STAMP(5);
#define EX(s, a, b) _Pragma("unroll") for (int i_ = (a); i_ < (b); ++i_) s[i_] = EXP2(s[i_]); SBAR()
    auto attn = [&](int t0, int t1) {
        for (int tr = t0; tr < t1; ++tr) {
            const unsigned son = tsoff(tr + 1);
            if (tr == ELOW && !kh) {
                __builtin_amdgcn_s_setprio(0);
                if (wid == 0) wait_flags((GAS u64*)(ws + WS_F2 + (oc * 32 + r) * 256), tag);
            }
#pragma unroll
            for (int u = 0; u < 4; ++u) {
                EX(sa, 0, 2); ob = SMF(vb[(u + 3) & 3], hi8(sb), ob); SBAR(); LOADT((u + 3) & 3, u ? son : soc); SBAR();
                EX(sa, 2, 5); sb = MFMA32(kf[u], qb, nmb); SBAR();
                EX(sa, 5, 11); oa = SMF(va[u], lo8(sa), oa); SBAR();
                EX(sa, 11, 16);
                EX(sb, 0, 2); oa = SMF(vb[u], hi8(sa), oa); SBAR();
                EX(sb, 2, 5); sa = MFMA32(kf[(u + 1) & 3], qa, nma); SBAR();
                EX(sb, 5, 11); ob = SMF(va[u], lo8(sb), ob); SBAR();
                EX(sb, 11, 16);
            }
            soc = son;
        }
    };
    const int ctrip = kh ? LTRIP : 0;
    attn(0, ctrip);
    STAMP(6);
    {
        __builtin_amdgcn_s_setprio(CPRIO);
        const int c = wid & 1, e = (wid >> 1) & 1, dlt = 16 * kh + 8 * e + 7;
        const int kap = (r + 1) & 3;
        unsigned ap = L_LO + c * LOC + kap * KCP + (2047 - r + kap + 8 * hh - 32 * dlt) * 2 - 2048;
        unsigned bp = L_UB + c * UBC + (32 + r - dlt) * 80 + hh * 16;
        const unsigned pA = L_PA + (c * 2 + e) * 8192 + lane * 16;
        f32x16 y0 = zero16, y1 = zero16;
        lds_wait(L_CTL + C_U, 4);
        if (kh) {
            lds_wait(L_CTL + C_E + 4 * (c * 2 + e), 1);
#pragma unroll
            for (int i = 0; i < 4; ++i) { const f32x4 p = ldsr<f32x4>(pA + i * 1024), q = ldsr<f32x4>(pA + 4096 + i * 1024);
#pragma unroll
                for (int k = 0; k < 4; ++k) { y0[4 * i + k] = p[k]; y1[4 * i + k] = q[k]; } }
        }
        struct Fr { u32x2 a0, a1, z0, z1; bf16x8 b0, b1; } f[2];
#define CA(i) (ap + 32 * (i))
#define CB(i) (bp + 80 * ((i) >> 1) + 32 * ((i) & 1))
#define CAT(x, y) __builtin_bit_cast(bf16x8, u32x4{x[0], x[1], y[0], y[1]})
        f[0].a0 = ldsr<u32x2>(CA(0) + 2048); f[0].a1 = ldsr<u32x2>(CA(0) + 2056); f[0].b0 = ldsr<bf16x8>(CB(0));
        f[0].b1 = ldsr<bf16x8>(CB(0) + 2560); f[0].z0 = ldsr<u32x2>(CA(0)); f[0].z1 = ldsr<u32x2>(CA(0) + 8);
        SBAR();
#pragma unroll
        for (int i = 0; i < 16; ++i) {
            Fr& q = f[i & 1]; Fr& n = f[(i + 1) & 1];
            y0 = MFMA32(CAT(q.a0, q.a1), q.b0, y0); SBAR();
            if (i < 15) { n.a0 = ldsr<u32x2>(CA(i + 1) + 2048); n.a1 = ldsr<u32x2>(CA(i + 1) + 2056); n.b0 = ldsr<bf16x8>(CB(i + 1)); } SBAR();
            y1 = MFMA32(CAT(q.a0, q.a1), q.b1, y1); SBAR();
            if (i < 15) { n.b1 = ldsr<bf16x8>(CB(i + 1) + 2560); n.z0 = ldsr<u32x2>(CA(i + 1)); n.z1 = ldsr<u32x2>(CA(i + 1) + 8); } SBAR();
            y1 = MFMA32(CAT(q.z0, q.z1), q.b0, y1); SBAR();
        }
        STAMP(27);
#pragma unroll
        for (int i = 0; i < 4; ++i) {
            if (!kh || e) ldsw<f32x4>(pA + i * 1024, f32x4{y0[4 * i], y0[4 * i + 1], y0[4 * i + 2], y0[4 * i + 3]});
            if (!kh || !e) ldsw<f32x4>(pA + 4096 + i * 1024, f32x4{y1[4 * i], y1[4 * i + 1], y1[4 * i + 2], y1[4 * i + 3]});
        }
        asm volatile("s_waitcnt lgkmcnt(0)" ::: "memory");
        if (lane == 0) lds_add(L_CTL + C_E + 4 * (c * 2 + e));
        if (kh) {
            lds_wait(L_CTL + C_E + 4 * (c * 2 + 1 - e), 2);
            f32x16 y = e ? y1 : y0;
            const unsigned pO = L_PA + (c * 2 + 1 - e) * 8192 + e * 4096 + lane * 16;
#pragma unroll
            for (int i = 0; i < 4; ++i) { const f32x4 p = ldsr<f32x4>(pO + i * 1024);
#pragma unroll
                for (int k = 0; k < 4; ++k) y[4 * i + k] += p[k]; }
            const float dsk = c ? bfhi(dk2) : bf2f(dk2 & 0xffff);
            u32x2 w[4];
#pragma unroll
            for (int i = 0; i < 4; ++i) {
                const u32x2 u = ldsr<u32x2>(L_UB + c * UBC + (32 + 32 * e + r) * 80 + i * 16 + hh * 8);
                const unsigned q0 = pk2(y[4 * i], y[4 * i + 1]), q1 = pk2(y[4 * i + 2], y[4 * i + 3]);
                const unsigned s0 = pk2(bf2f(u[0] & 0xffff) * dsk, bfhi(u[0]) * dsk), s1 = pk2(bf2f(u[1] & 0xffff) * dsk, bfhi(u[1]) * dsk);
                w[i] = u32x2{pk2(bf2f(q0 & 0xffff) + bf2f(s0 & 0xffff), bfhi(q0) + bfhi(s0)), pk2(bf2f(q1 & 0xffff) + bf2f(s1 & 0xffff), bfhi(q1) + bfhi(s1))};
            }
#pragma unroll
            for (int i = 0; i < 4; i += 2) {
                const auto s0 = __builtin_amdgcn_permlane32_swap(w[i][0], w[i + 1][0], false, false), s1 = __builtin_amdgcn_permlane32_swap(w[i][1], w[i + 1][1], false, false);
                __builtin_amdgcn_raw_buffer_store_b128(u32x4{s0[0], s1[0], s0[1], s1[1]}, rs, WS_BT + (c0 + c) * 4096 + (32 * e + r) * 64 + i * 16 + hh * 16, 0, SC1);
            }
            asm volatile("s_waitcnt vmcnt(0) lgkmcnt(0)" ::: "memory");
            if (lane == 0 && lds_add(L_CTL + C_CONV) == 3u) __hip_atomic_store((GAS u64*)(ws + WS_F2 + (hg * 32 + rt) * 256), tag, RLX_AGENT);
        } else {
            const unsigned vo = (128 * wid + (lane >> 3)) * 1024 + (((((lane & 7) >> 1) ^ g) * 2 + (lane & 1)) << 4);
            const char* s = (const char*)P.wo + oc * 128;
#pragma unroll
            for (int i = 0; i < 16; ++i) glds_s(s + i * 8192, vo, wid * 16384 + i * 1024);
        }
        __builtin_amdgcn_s_setprio(1);
    }
    STAMP(7);
    attn(ctrip, 8);
    ob = SMF(vb[3], hi8(sb), ob);
    STAMP(8);
    {
        const unsigned xw = L_XB + wid * XBW, xr = L_XB + (wid ^ 4) * XBW;
        const f32x16 os = kh ? oa : ob; f32x16 o = kh ? ob : oa;
        const float ms = kh ? nma[0] : nmb[0], mk = kh ? nmb[0] : nma[0];
        lds_wait(L_CTL + C_CONV, 4);
        ldsw<f32x4>(xw + lane * 16, f32x4{os[0], os[1], os[2], os[3]}); ldsw<f32x4>(xw + 1024 + lane * 16, f32x4{os[4], os[5], os[6], os[7]});
        ldsw<f32x2>(xw + 2048 + lane * 8, f32x2{os[8], ms});
        STAMP(30); BAR();
        const u32x4 bv = __builtin_amdgcn_raw_buffer_load_b128(rs, WS_BT + (64 * oc + (tid >> 3)) * 4096 + ort * 128 + (tid & 7) * 16, 0, SC1);
        SBAR();
        const f32x4 p0 = ldsr<f32x4>(xr + lane * 16), p1 = ldsr<f32x4>(xr + 1024 + lane * 16); const f32x2 p2 = ldsr<f32x2>(xr + 2048 + lane * 8);
        const float mn = fminf(mk, p2[1]), f0 = EXP2(mn - mk), f1 = EXP2(mn - p2[1]);
        float l = o[8] * f0 + p2[0] * f1;
        const bool safe = o[8] < 0x1p60f && p2[0] < 0x1p60f && l > 0x1p-30f;
#pragma unroll
        for (int i = 0; i < 4; ++i) { o[i] = o[i] * f0 + p0[i] * f1; o[4 + i] = o[4 + i] * f0 + p1[i] * f1; }
        if (!__all(safe)) {
            const bf16x8 q = kh ? qb : qa; float m = -INFINITY; o = zero16;
            for (int t = 0; t < 64; ++t) {
                const unsigned so = kvh + t * KVT;
                const bf16x8 k = __builtin_bit_cast(bf16x8, __builtin_amdgcn_raw_buffer_load_b128(rs, voffK, so, SC1));
                const bf16x8 v0 = __builtin_bit_cast(bf16x8, __builtin_amdgcn_raw_buffer_load_b128(rs, voffV, so, SC1));
                const bf16x8 v1 = __builtin_bit_cast(bf16x8, __builtin_amdgcn_raw_buffer_load_b128(rs, voffV + 640, so, SC1));
                f32x16 s = MFMA32(k, q, zero16);
                const float m2 = fmaxf(m, rowmax(s)), al = EXP2(m - m2); m = m2;
#pragma unroll
                for (int i = 0; i < 16; ++i) { s[i] = EXP2(s[i] - m); o[i] *= al; }
                o = SMF(v0, lo8(s), o); o = SMF(v1, hi8(s), o);
            }
            l = o[8];
        }
        const float rl = 1.0f / l;
        __builtin_amdgcn_raw_buffer_store_b128(u32x4{pk2(o[0] * rl, o[1] * rl), pk2(o[2] * rl, o[3] * rl), pk2(o[4] * rl, o[5] * rl), pk2(o[6] * rl, o[7] * rl)}, rs,
                                               WS_OF + ((rt * 4 + 2 * kh + (r >> 4)) * 16 + 2 * hg + (hd >> 1)) * 1024 + (2 * (hd & 1) + hh) * 256 + r16 * 16, 0, SC1);
        ldsw<u32x4>(L_BI + (tid >> 3) * BROW + (tid & 7) * 16, bv);
    }
    VBAR(0);
    STAMP(9);
    if (wid == 0) {
        if (lane == 0) __hip_atomic_store((GAS u64*)(ws + WS_F3 + (rt * 8 + hg) * 256), tag, RLX_AGENT);
    }
    const int rg = wid & 3;
    bf16x8 wf[8][4];
#pragma unroll
    for (int m = 0; m < 4; ++m) {
        const unsigned a = (256 * kh + 16 * (g >> 1) + 4 * (g & 1) + (r16 >> 2)) * 128 + ((m ^ (2 * (g & 1) + (r16 >> 3))) << 5) + (lane & 3) * 8;
#pragma unroll
        for (int ks = 0; ks < 8; ++ks) wf[ks][m] = trfrag(a + ks * 4096, a + ks * 4096 + 1024);
    }
    if (wid == 0) {
        wait_flags((GAS u64*)(ws + WS_F3 + (ort * 8 + (lane & 7)) * 256), tag);
        if (bid == 0 && lane == 0) __hip_atomic_store((GAS u64*)(ws + WS_NONCE), tag, RLX_AGENT);
    }
    BAR();
    STAMP(10);
    {
        const float gate = __uint_as_float(__builtin_amdgcn_raw_buffer_load_b32(rs, WS_G + (ort * 64 + 16 * rg + r16) * 4, 0, SC1));
        bf16x8 of[8];
#pragma unroll
        for (int ks = 0; ks < 8; ++ks) of[ks] = __builtin_bit_cast(bf16x8, __builtin_amdgcn_raw_buffer_load_b128(rs, WS_OF + ((ort * 4 + rg) * 16 + 8 * kh + ks) * 1024 + lane * 16, 0, SC1));
        SBAR();
        STAMP(28);
        f32x4 acc[4];
#pragma unroll
        for (int m = 0; m < 4; ++m) acc[m] = f32x4{0.f, 0.f, 0.f, 0.f};
#pragma unroll
        for (int ks = 0; ks < 8; ++ks)
#pragma unroll
            for (int m = 0; m < 4; ++m) acc[m] = MFMA16(wf[ks][m], of[ks], acc[m]);
        STAMP(29);
        const f32x4 k0 = kh ? acc[2] : acc[0], k1 = kh ? acc[3] : acc[1], s0 = kh ? acc[0] : acc[2], s1 = kh ? acc[1] : acc[3];
        ldsw<f32x4>(L_RED + wid * 2048 + lane * 16, s0); ldsw<f32x4>(L_RED + wid * 2048 + 1024 + lane * 16, s1);
        BAR();
        STAMP(11);
        const float omg = rbf(1.0f - gate);
#pragma unroll
        for (int e = 0; e < 2; ++e) {
            const f32x4 a = (e ? k1 : k0) + ldsr<f32x4>(L_RED + (wid ^ 4) * 2048 + e * 1024 + lane * 16);
            const u32x2 b = trread(L_BI + (16 * (2 * kh + e) + 4 * g + (r16 >> 2)) * BROW + rg * 32 + (lane & 3) * 8);
            const unsigned a0 = pk2(a[0], a[1]), a1 = pk2(a[2], a[3]);
            const unsigned ga0 = pk2(gate * bf2f(a0 & 0xffff), gate * bfhi(a0)), ga1 = pk2(gate * bf2f(a1 & 0xffff), gate * bfhi(a1));
            const unsigned gb0 = pk2(omg * bf2f(b[0] & 0xffff), omg * bfhi(b[0])), gb1 = pk2(omg * bf2f(b[1] & 0xffff), omg * bfhi(b[1]));
            *(u32x2*)(P.out + (ort * 64 + 16 * rg + r16) * 512 + 64 * oc + 16 * (2 * kh + e) + 4 * g) =
                u32x2{pk2(bf2f(ga0 & 0xffff) + bf2f(gb0 & 0xffff), bfhi(ga0) + bfhi(gb0)), pk2(bf2f(ga1 & 0xffff) + bf2f(gb1 & 0xffff), bfhi(ga1) + bfhi(gb1))};
        }
    }
    STAMP(12); STAMP_OUT();
}

__global__ void __launch_bounds__(64) probe_kernel(char* ws) {
    const u64 t0 = __builtin_amdgcn_s_memrealtime();
    for (int z = 0; z < 100000 && __builtin_amdgcn_s_memrealtime() < t0 + 3000; ++z) __builtin_amdgcn_s_sleep(2);
    if (threadIdx.x == 0) __hip_atomic_store((GAS u64*)(ws + WS_STAMP), (u64)__builtin_amdgcn_s_memrealtime(), RLX_AGENT);
}
__global__ void __launch_bounds__(64) probe2_kernel(char* ws) {
    const u64 pe = __hip_atomic_load((GAS u64*)(ws + WS_STAMP), RLX_AGENT);
    const u64 t0 = __builtin_amdgcn_s_memrealtime();
    const u64 lim = pe + 10000 + 0 * (t0 - pe);
    for (int z = 0; z < 200000 && __builtin_amdgcn_s_memrealtime() < lim; ++z) __builtin_amdgcn_s_sleep(2);
}
extern "C" void kernel_launch(void* const* d_in, const int* in_sizes, int n_in, void* d_out, int out_size, void* d_ws, size_t ws_size, hipStream_t stream) {
    Params p;
    p.x = (const u16*)d_in[0]; p.wqkv = (const u16*)d_in[1]; p.wo = (const u16*)d_in[2];
    p.are = (const u16*)d_in[4]; p.aim = (const u16*)d_in[5]; p.bre = (const u16*)d_in[6]; p.bim = (const u16*)d_in[7];
    p.cre = (const u16*)d_in[8]; p.cim = (const u16*)d_in[9]; p.logdt = (const u16*)d_in[10]; p.dskip = (const u16*)d_in[11]; p.gw = (const u16*)d_in[12];
    p.out = (u16*)d_out; p.ws = (char*)d_ws;
    (void)hipFuncSetAttribute((const void*)fused_kernel, hipFuncAttributeMaxDynamicSharedMemorySize, LDS_BYTES);
    probe_kernel<<<1, 64, 0, stream>>>((char*)d_ws);
    fused_kernel<<<256, 512, LDS_BYTES, stream>>>(p);
    probe2_kernel<<<1, 64, 0, stream>>>((char*)d_ws);
}
```

```cpp
#include <hip/hip_runtime.h>
#include <hip/hip_bf16.h>
#include <stdint.h>

typedef float f32x2 __attribute__((ext_vector_type(2)));
typedef float f32x4 __attribute__((ext_vector_type(4)));
typedef float f32x8 __attribute__((ext_vector_type(8)));
typedef float f32x16 __attribute__((ext_vector_type(16)));
typedef __bf16 bf16x2 __attribute__((ext_vector_type(2)));
typedef __bf16 bf16x8 __attribute__((ext_vector_type(8)));
typedef __bf16 bf16x16 __attribute__((ext_vector_type(16)));
typedef short s16x4 __attribute__((ext_vector_type(4)));
typedef unsigned u32x2 __attribute__((ext_vector_type(2)));
typedef unsigned u32x4 __attribute__((ext_vector_type(4)));
typedef unsigned short u16;
typedef unsigned long long u64;
#define LAS __attribute__((address_space(3)))
#define GAS __attribute__((address_space(1)))
#define CAS __attribute__((address_space(4)))
#define RLX_AGENT __ATOMIC_RELAXED, __HIP_MEMORY_SCOPE_AGENT
#define SC1 16
#define NA 1
#define LTRIP 4
#define AHEAD 3
#define CODE_LINES 6
#define WARMX 1
#define LPRIO 3
#define TLATE 0
#define KPRIO 3
#define LPRE 1
#define YTRIP 6
#define LOSPL 4
#define WV 4
#define GPRIO 2
#define CVP 3
#define SEARLY 1
#define ORD 3
#define WDEL 0
#define EPRE 2
#define ELOW 7
#define ROT (rt >> 1)
#define CPRIO 3

constexpr unsigned WS_NONCE = 0, WS_F1 = 4096, WS_F2 = WS_F1 + 65536, WS_F3 = WS_F2 + 65536;
constexpr unsigned WS_F0 = WS_F1 + 128;
constexpr unsigned WS_G = WS_F3 + 65536;
constexpr unsigned WS_KV = 262144, KVT = 2304;
constexpr unsigned WS_XT = WS_KV + 32 * 64 * KVT;
constexpr unsigned WS_BT = WS_XT + 2097152;
constexpr unsigned WS_OF = WS_BT + 2097152;
constexpr unsigned WS_END = WS_OF + 2097152;
constexpr unsigned WS_STAMP = 16u << 20;
constexpr unsigned SLOT = 32768, TROW = 528;
constexpr unsigned slot_at(int c) { return (c & 3) * SLOT; }
constexpr unsigned L_LO = 64, LOC = 17152;
constexpr unsigned L_HI = 34368, HIC = 27 * TROW;
constexpr unsigned L_WO = 65536;
constexpr unsigned L_QX = 131072, L_SH = 139264;
constexpr unsigned L_UB = 141312, UBC = 7680;
constexpr unsigned L_CTL = 163712, LDS_BYTES = 163840;
constexpr unsigned KCP = 2368;
constexpr unsigned L_DT = 156672, DTC = 3072;
constexpr unsigned L_WS = 65536, WSC = 4352;
constexpr unsigned L_PA = 34368;
constexpr unsigned L_XB = 0, XBW = 2560;
constexpr unsigned L_BI = 141312, BROW = 144, L_RED = 34368;
enum { C_KV = 0, C_WQ = 4, C_U = 8, C_CONV = 12, C_TAB = 16, C_R7 = 20, C_Q = 24, C_F = 28, C_E = 32, C_O = 48, C_K0 = 56, C_M = 64, C_LO = 96 };
constexpr float QSCALE = 0.25f * 1.4426950408889634f;
constexpr int SMF_IDX = (int)0xDDDDDDDD;

__device__ __forceinline__ float bf2f(unsigned v) { return __uint_as_float(v << 16); }
__device__ __forceinline__ float bfhi(unsigned v) { return __uint_as_float(v & 0xffff0000u); }
__device__ __forceinline__ unsigned pk2(float lo, float hi) { bf16x2 v = {(__bf16)lo, (__bf16)hi}; return __builtin_bit_cast(unsigned, v); }
__device__ __forceinline__ float rbf(float v) { return bf2f(__builtin_bit_cast(u16, (__bf16)v)); }
__device__ __forceinline__ u32x2 trread(unsigned a) { return __builtin_bit_cast(u32x2, __builtin_amdgcn_ds_read_tr16_b64_v4i16((LAS s16x4*)a)); }
__device__ __forceinline__ bf16x8 trfrag(unsigned a0, unsigned a1) { const u32x2 a = trread(a0), b = trread(a1); return __builtin_bit_cast(bf16x8, u32x4{a[0], a[1], b[0], b[1]}); }
template <typename T> __device__ __forceinline__ T ldsr(unsigned a) { return *(LAS const T*)a; }
template <typename T> __device__ __forceinline__ void ldsw(unsigned a, T v) { *(LAS T*)a = v; }
__device__ __forceinline__ void glds_s(const void* sbase, unsigned voff, unsigned lds) {
    asm volatile("s_mov_b32 m0, %2\n\ts_nop 0\n\tglobal_load_lds_dwordx4 %0, %1" :: "v"(voff), "s"(sbase), "s"(lds) : "memory");
}
__device__ __forceinline__ void glds_v(const void* vaddr, unsigned lds) {
    asm volatile("s_mov_b32 m0, %1\n\ts_nop 0\n\tglobal_load_lds_dwordx4 %0, off" :: "v"(vaddr), "s"(lds) : "memory");
}
#define BAR() asm volatile("s_waitcnt lgkmcnt(0)\n\ts_barrier" ::: "memory")
#define VBAR(n) asm volatile("s_waitcnt vmcnt(" #n ") lgkmcnt(0)\n\ts_barrier" ::: "memory")
#define SBAR() __builtin_amdgcn_sched_barrier(0)
#define MFMA32(a, b, c) __builtin_amdgcn_mfma_f32_32x32x16_bf16(a, b, c, 0, 0, 0)
#define MFMA16(a, b, c) __builtin_amdgcn_mfma_f32_16x16x32_bf16(a, b, c, 0, 0, 0)
#define SMF(a, p, o) __builtin_amdgcn_smfmac_f32_32x32x32_bf16(a, __builtin_bit_cast(bf16x16, p), o, SMF_IDX, 0, 0)
#define EXP2(v) __builtin_amdgcn_exp2f(v)
#define STAMP(i)
#define STAMP_DECL
#define STAMP_OUT()

struct C2 { f32x2 m0, m1; };
__device__ __forceinline__ f32x2 cmul(f32x2 a, f32x2 b) {
    const f32x2 t = a * f32x2{b[0], b[0]};
    return __builtin_elementwise_fma(f32x2{a[1], a[0]}, f32x2{-b[1], b[1]}, t);
}
__device__ __forceinline__ C2 cmul2(const C2& a, const C2& b) { return C2{cmul(a.m0, b.m0), cmul(a.m1, b.m1)}; }
__device__ __forceinline__ C2 cmul2s(const C2& a, const C2& b) {
    float r0, i0, r1, i1;
    asm("v_mul_f32 %0, %4, %8\n\tv_mul_f32 %1, %5, %8\n\tv_mul_f32 %2, %6, %10\n\tv_mul_f32 %3, %7, %10\n\t"
        "v_fma_f32 %0, -%5, %9, %0\n\tv_fma_f32 %1, %4, %9, %1\n\tv_fma_f32 %2, -%7, %11, %2\n\tv_fma_f32 %3, %6, %11, %3"
        : "=&v"(r0), "=&v"(i0), "=&v"(r1), "=&v"(i1) : "v"(a.m0[0]), "v"(a.m0[1]), "v"(a.m1[0]), "v"(a.m1[1]), "v"(b.m0[0]), "v"(b.m0[1]), "v"(b.m1[0]), "v"(b.m1[1]));
    return C2{f32x2{r0, i0}, f32x2{r1, i1}};
}
__device__ __forceinline__ u32x2 cpack(const C2& a) { return u32x2{pk2(a.m0[0], a.m0[1]), pk2(a.m1[0], a.m1[1])}; }
__device__ __forceinline__ f32x2 cexpm(float al2, float rev, float mult) {
    const float e = EXP2(al2 * mult), f = __builtin_amdgcn_fractf(rev * mult);
    return f32x2{e * __builtin_amdgcn_cosf(f), e * __builtin_amdgcn_sinf(f)};
}
__device__ __forceinline__ C2 cexp2(float l0, float v0, float l1, float v1, float mult) { return C2{cexpm(l0, v0, mult), cexpm(l1, v1, mult)}; }
__device__ __forceinline__ void wait_flags(GAS u64* p, u64 tag) {
#pragma unroll 1
    for (int spins = 0; spins < (1 << 18); ++spins) {
        if (__all(__hip_atomic_load(p, RLX_AGENT) == tag)) break;
        __builtin_amdgcn_s_sleep(2);
    }
}
__device__ __forceinline__ f32x8 lo8(const f32x16& s) { return __builtin_shufflevector(s, s, 0, 1, 2, 3, 4, 5, 6, 7); }
__device__ __forceinline__ f32x8 hi8(const f32x16& s) { return __builtin_shufflevector(s, s, 8, 9, 10, 11, 12, 13, 14, 15); }
__device__ __forceinline__ float rowmax(const f32x16& s) {
    float m = fmaxf(fmaxf(s[0], s[1]), s[2]);
#pragma unroll
    for (int i = 3; i < 15; i += 2) m = fmaxf(fmaxf(m, s[i]), s[i + 1]);
    m = fmaxf(m, s[15]);
    const auto r = __builtin_amdgcn_permlane32_swap(__float_as_uint(m), __float_as_uint(m), false, false);
    return fmaxf(__uint_as_float(r[0]), __uint_as_float(r[1]));
}

typedef float f32x32 __attribute__((ext_vector_type(32)));
typedef unsigned u32x8 __attribute__((ext_vector_type(8)));
typedef unsigned u32x16 __attribute__((ext_vector_type(16)));
typedef unsigned u32x32 __attribute__((ext_vector_type(32)));
#define ATTN_PLAIN \
    "s_nop 15\n\tL%=:\n\tv_exp_f32 v32, v32\n\tv_exp_f32 v33, v33\n\t" \
    "v_smfmac_f32_32x32x32_bf16 v[16:31], v[152:155], v[56:63], v160\n\tbuffer_load_dwordx4 v[148:151], v111, %[rs], %[soc] offen sc1\n\tbuffer_load_dwordx4 v[152:155], v111, %[rs], %[soc] offen offset:640 sc1\n\tbuffer_load_dwordx4 v[156:159], v107, %[rs], %[soc] offen sc1\n\t" \
    "v_exp_f32 v34, v34\n\ts_and_b32 %[son], %[k], 7\n\ts_mulk_i32 %[son], 0x2400\n\tv_exp_f32 v35, v35\n\t" \
    "s_add_u32 %[son], %[son], %[base]\n\ts_cmp_eq_u32 %[k], %[lim]\n\tv_exp_f32 v36, v36\n\tv_mfma_f32_32x32x16_bf16 v[48:63], v[120:123], v[100:103], v[64:79]\n\t" \
    "v_exp_f32 v37, v37\n\ts_cselect_b32 %[son], 0xac0000, %[son]\n\ts_add_u32 %[k], %[k], 1\n\tv_exp_f32 v38, v38\n\t" \
    "v_exp_f32 v39, v39\n\tv_exp_f32 v40, v40\n\tv_exp_f32 v41, v41\n\tv_exp_f32 v42, v42\n\t" \
    "v_smfmac_f32_32x32x32_bf16 v[0:15], v[112:115], v[32:39], v160\n\tv_exp_f32 v43, v43\n\tv_exp_f32 v44, v44\n\tv_exp_f32 v45, v45\n\t" \
    "v_exp_f32 v46, v46\n\tv_exp_f32 v47, v47\n\tv_exp_f32 v48, v48\n\tv_exp_f32 v49, v49\n\t" \
    "v_smfmac_f32_32x32x32_bf16 v[0:15], v[116:119], v[40:47], v160\n\tv_exp_f32 v50, v50\n\tv_exp_f32 v51, v51\n\tv_exp_f32 v52, v52\n\t" \
    "s_waitcnt vmcnt(6)\n\tv_mfma_f32_32x32x16_bf16 v[32:47], v[132:135], v[96:99], v[64:79]\n\tv_exp_f32 v53, v53\n\tv_exp_f32 v54, v54\n\t" \
    "v_exp_f32 v55, v55\n\tv_exp_f32 v56, v56\n\tv_exp_f32 v57, v57\n\tv_exp_f32 v58, v58\n\t" \
    "v_smfmac_f32_32x32x32_bf16 v[16:31], v[112:115], v[48:55], v160\n\tv_exp_f32 v59, v59\n\tv_exp_f32 v60, v60\n\tv_exp_f32 v61, v61\n\t" \
    "v_exp_f32 v62, v62\n\tv_exp_f32 v63, v63\n\tv_exp_f32 v32, v32\n\tv_exp_f32 v33, v33\n\t" \
    "v_smfmac_f32_32x32x32_bf16 v[16:31], v[116:119], v[56:63], v160\n\tbuffer_load_dwordx4 v[112:115], v108, %[rs], %[son] offen sc1\n\tbuffer_load_dwordx4 v[116:119], v108, %[rs], %[son] offen offset:640 sc1\n\tbuffer_load_dwordx4 v[120:123], v104, %[rs], %[son] offen sc1\n\t" \
    "v_exp_f32 v34, v34\n\tv_exp_f32 v35, v35\n\tv_exp_f32 v36, v36\n\tv_mfma_f32_32x32x16_bf16 v[48:63], v[132:135], v[100:103], v[64:79]\n\t" \
    "v_exp_f32 v37, v37\n\tv_exp_f32 v38, v38\n\tv_exp_f32 v39, v39\n\tv_exp_f32 v40, v40\n\t" \
    "v_exp_f32 v41, v41\n\tv_exp_f32 v42, v42\n\tv_smfmac_f32_32x32x32_bf16 v[0:15], v[124:127], v[32:39], v160\n\tv_exp_f32 v43, v43\n\t" \
    "v_exp_f32 v44, v44\n\tv_exp_f32 v45, v45\n\tv_exp_f32 v46, v46\n\tv_exp_f32 v47, v47\n\t" \
    "v_exp_f32 v48, v48\n\tv_exp_f32 v49, v49\n\tv_smfmac_f32_32x32x32_bf16 v[0:15], v[128:131], v[40:47], v160\n\tv_exp_f32 v50, v50\n\t" \
    "v_exp_f32 v51, v51\n\tv_exp_f32 v52, v52\n\ts_waitcnt vmcnt(6)\n\tv_mfma_f32_32x32x16_bf16 v[32:47], v[144:147], v[96:99], v[64:79]\n\t" \
    "v_exp_f32 v53, v53\n\tv_exp_f32 v54, v54\n\tv_exp_f32 v55, v55\n\tv_exp_f32 v56, v56\n\t" \
    "v_exp_f32 v57, v57\n\tv_exp_f32 v58, v58\n\tv_smfmac_f32_32x32x32_bf16 v[16:31], v[124:127], v[48:55], v160\n\tv_exp_f32 v59, v59\n\t" \
    "v_exp_f32 v60, v60\n\tv_exp_f32 v61, v61\n\tv_exp_f32 v62, v62\n\tv_exp_f32 v63, v63\n\t" \
    "v_exp_f32 v32, v32\n\tv_exp_f32 v33, v33\n\tv_smfmac_f32_32x32x32_bf16 v[16:31], v[128:131], v[56:63], v160\n\tbuffer_load_dwordx4 v[124:127], v109, %[rs], %[son] offen sc1\n\t" \
    "buffer_load_dwordx4 v[128:131], v109, %[rs], %[son] offen offset:640 sc1\n\tbuffer_load_dwordx4 v[132:135], v105, %[rs], %[son] offen sc1\n\tv_exp_f32 v34, v34\n\tv_exp_f32 v35, v35\n\t" \
    "v_exp_f32 v36, v36\n\tv_mfma_f32_32x32x16_bf16 v[48:63], v[144:147], v[100:103], v[64:79]\n\tv_exp_f32 v37, v37\n\tv_exp_f32 v38, v38\n\t" \
    "v_exp_f32 v39, v39\n\tv_exp_f32 v40, v40\n\tv_exp_f32 v41, v41\n\tv_exp_f32 v42, v42\n\t" \
    "v_smfmac_f32_32x32x32_bf16 v[0:15], v[136:139], v[32:39], v160\n\tv_exp_f32 v43, v43\n\tv_exp_f32 v44, v44\n\tv_exp_f32 v45, v45\n\t" \
    "v_exp_f32 v46, v46\n\tv_exp_f32 v47, v47\n\tv_exp_f32 v48, v48\n\tv_exp_f32 v49, v49\n\t" \
    "v_smfmac_f32_32x32x32_bf16 v[0:15], v[140:143], v[40:47], v160\n\tv_exp_f32 v50, v50\n\tv_exp_f32 v51, v51\n\tv_exp_f32 v52, v52\n\t" \
    "s_waitcnt vmcnt(6)\n\tv_mfma_f32_32x32x16_bf16 v[32:47], v[156:159], v[96:99], v[64:79]\n\tv_exp_f32 v53, v53\n\tv_exp_f32 v54, v54\n\t" \
    "v_exp_f32 v55, v55\n\tv_exp_f32 v56, v56\n\tv_exp_f32 v57, v57\n\tv_exp_f32 v58, v58\n\t" \
    "v_smfmac_f32_32x32x32_bf16 v[16:31], v[136:139], v[48:55], v160\n\tv_exp_f32 v59, v59\n\tv_exp_f32 v60, v60\n\tv_exp_f32 v61, v61\n\t" \
    "v_exp_f32 v62, v62\n\tv_exp_f32 v63, v63\n\ts_cmp_eq_u32 %[k], %[yk]\n\ts_cbranch_scc0 Y%=\n\t" \
    "s_setprio 0\n\tY%=:\n\tv_exp_f32 v32, v32\n\tv_exp_f32 v33, v33\n\t" \
    "v_smfmac_f32_32x32x32_bf16 v[16:31], v[140:143], v[56:63], v160\n\tbuffer_load_dwordx4 v[136:139], v110, %[rs], %[son] offen sc1\n\tbuffer_load_dwordx4 v[140:143], v110, %[rs], %[son] offen offset:640 sc1\n\tbuffer_load_dwordx4 v[144:147], v106, %[rs], %[son] offen sc1\n\t" \
    "v_exp_f32 v34, v34\n\tv_exp_f32 v35, v35\n\tv_exp_f32 v36, v36\n\tv_mfma_f32_32x32x16_bf16 v[48:63], v[156:159], v[100:103], v[64:79]\n\t" \
    "v_exp_f32 v37, v37\n\tv_exp_f32 v38, v38\n\tv_exp_f32 v39, v39\n\tv_exp_f32 v40, v40\n\t" \
    "v_exp_f32 v41, v41\n\tv_exp_f32 v42, v42\n\tv_smfmac_f32_32x32x32_bf16 v[0:15], v[148:151], v[32:39], v160\n\tv_exp_f32 v43, v43\n\t" \
    "v_exp_f32 v44, v44\n\tv_exp_f32 v45, v45\n\tv_exp_f32 v46, v46\n\tv_exp_f32 v47, v47\n\t" \
    "v_exp_f32 v48, v48\n\tv_exp_f32 v49, v49\n\tv_smfmac_f32_32x32x32_bf16 v[0:15], v[152:155], v[40:47], v160\n\tv_exp_f32 v50, v50\n\t" \
    "v_exp_f32 v51, v51\n\tv_exp_f32 v52, v52\n\ts_waitcnt vmcnt(6)\n\tv_mfma_f32_32x32x16_bf16 v[32:47], v[120:123], v[96:99], v[64:79]\n\t" \
    "v_exp_f32 v53, v53\n\tv_exp_f32 v54, v54\n\tv_exp_f32 v55, v55\n\tv_exp_f32 v56, v56\n\t" \
    "v_exp_f32 v57, v57\n\tv_exp_f32 v58, v58\n\tv_smfmac_f32_32x32x32_bf16 v[16:31], v[148:151], v[48:55], v160\n\tv_exp_f32 v59, v59\n\t" \
    "v_exp_f32 v60, v60\n\ts_mov_b32 %[soc], %[son]\n\ts_sub_u32 %[n], %[n], 1\n\tv_exp_f32 v61, v61\n\t" \
    "v_exp_f32 v62, v62\n\tv_exp_f32 v63, v63\n\ts_cmp_lg_u32 %[n], 0\n\ts_cbranch_scc1 L%=\n\t" \
    "s_nop 15\n\t"

#define CONV_LOOP \
    "ds_read_b64 v[112:113], v226 offset:0\n\tds_read_b64 v[114:115], v226 offset:8\n\tds_read_b128 v[116:119], v227 offset:0\n\tds_read_b128 v[120:123], v227 offset:2560\n\t" \
    "ds_read_b64 v[124:125], v226 offset:32\n\tds_read_b64 v[126:127], v226 offset:40\n\tds_read_b128 v[128:131], v227 offset:32\n\tds_read_b128 v[132:135], v227 offset:2592\n\t" \
    "ds_read_b64 v[136:137], v226 offset:64\n\tds_read_b64 v[138:139], v226 offset:72\n\tds_read_b128 v[140:143], v227 offset:80\n\tds_read_b128 v[144:147], v227 offset:2640\n\t" \
    "C%=:\n\ts_waitcnt lgkmcnt(8)\n\tv_mfma_f32_32x32x16_bf16 v[162:177], v[112:115], v[116:119], v[162:177]\n\tds_read_b64 v[148:149], v226 offset:96\n\t" \
    "ds_read_b64 v[150:151], v226 offset:104\n\tv_mfma_f32_32x32x16_bf16 v[178:193], v[112:115], v[120:123], v[178:193]\n\tds_read_b128 v[152:155], v227 offset:112\n\tds_read_b128 v[156:159], v227 offset:2672\n\t" \
    "s_waitcnt lgkmcnt(8)\n\tv_mfma_f32_32x32x16_bf16 v[162:177], v[124:127], v[128:131], v[162:177]\n\tds_read_b64 v[112:113], v226 offset:128\n\tds_read_b64 v[114:115], v226 offset:136\n\t" \
    "v_mfma_f32_32x32x16_bf16 v[178:193], v[124:127], v[132:135], v[178:193]\n\tds_read_b128 v[116:119], v227 offset:160\n\tds_read_b128 v[120:123], v227 offset:2720\n\ts_waitcnt lgkmcnt(8)\n\t" \
    "v_mfma_f32_32x32x16_bf16 v[162:177], v[136:139], v[140:143], v[162:177]\n\tds_read_b64 v[124:125], v226 offset:160\n\tds_read_b64 v[126:127], v226 offset:168\n\tv_mfma_f32_32x32x16_bf16 v[178:193], v[136:139], v[144:147], v[178:193]\n\t" \
    "ds_read_b128 v[128:131], v227 offset:192\n\tds_read_b128 v[132:135], v227 offset:2752\n\ts_waitcnt lgkmcnt(8)\n\tv_mfma_f32_32x32x16_bf16 v[162:177], v[148:151], v[152:155], v[162:177]\n\t" \
    "ds_read_b64 v[136:137], v226 offset:192\n\tds_read_b64 v[138:139], v226 offset:200\n\tv_mfma_f32_32x32x16_bf16 v[178:193], v[148:151], v[156:159], v[178:193]\n\tds_read_b128 v[140:143], v227 offset:240\n\t" \
    "ds_read_b128 v[144:147], v227 offset:2800\n\ts_waitcnt lgkmcnt(8)\n\tv_mfma_f32_32x32x16_bf16 v[162:177], v[112:115], v[116:119], v[162:177]\n\tds_read_b64 v[148:149], v226 offset:224\n\t" \
    "ds_read_b64 v[150:151], v226 offset:232\n\tv_mfma_f32_32x32x16_bf16 v[178:193], v[112:115], v[120:123], v[178:193]\n\tds_read_b128 v[152:155], v227 offset:272\n\tds_read_b128 v[156:159], v227 offset:2832\n\t" \
    "s_waitcnt lgkmcnt(8)\n\tv_mfma_f32_32x32x16_bf16 v[162:177], v[124:127], v[128:131], v[162:177]\n\tds_read_b64 v[112:113], v226 offset:256\n\tds_read_b64 v[114:115], v226 offset:264\n\t" \
    "v_mfma_f32_32x32x16_bf16 v[178:193], v[124:127], v[132:135], v[178:193]\n\tds_read_b128 v[116:119], v227 offset:320\n\tds_read_b128 v[120:123], v227 offset:2880\n\ts_waitcnt lgkmcnt(8)\n\t" \
    "v_mfma_f32_32x32x16_bf16 v[162:177], v[136:139], v[140:143], v[162:177]\n\tds_read_b64 v[124:125], v226 offset:288\n\tds_read_b64 v[126:127], v226 offset:296\n\tv_mfma_f32_32x32x16_bf16 v[178:193], v[136:139], v[144:147], v[178:193]\n\t" \
    "ds_read_b128 v[128:131], v227 offset:352\n\tds_read_b128 v[132:135], v227 offset:2912\n\ts_waitcnt lgkmcnt(8)\n\tv_mfma_f32_32x32x16_bf16 v[162:177], v[148:151], v[152:155], v[162:177]\n\t" \
    "ds_read_b64 v[136:137], v226 offset:320\n\tds_read_b64 v[138:139], v226 offset:328\n\tv_mfma_f32_32x32x16_bf16 v[178:193], v[148:151], v[156:159], v[178:193]\n\tds_read_b128 v[140:143], v227 offset:400\n\t" \
    "ds_read_b128 v[144:147], v227 offset:2960\n\tv_add_u32 v226, 0x100, v226\n\tv_add_u32 v227, 0x140, v227\n\ts_sub_u32 %[n], %[n], 1\n\t" \
    "s_cmp_lg_u32 %[n], 0\n\ts_cbranch_scc1 C%=\n\ts_cmp_lg_u32 %[e], 0\n\ts_cbranch_scc1 F%=\n\t" \
    "s_waitcnt lgkmcnt(0)\n\tds_read_b64 v[112:113], v228 offset:0\n\tds_read_b64 v[114:115], v228 offset:8\n\tds_read_b128 v[116:119], v229 offset:0\n\t" \
    "ds_read_b64 v[120:121], v228 offset:32\n\tds_read_b64 v[122:123], v228 offset:40\n\tds_read_b128 v[124:127], v229 offset:32\n\tds_read_b64 v[128:129], v228 offset:64\n\t" \
    "ds_read_b64 v[130:131], v228 offset:72\n\tds_read_b128 v[132:135], v229 offset:80\n\tds_read_b64 v[136:137], v228 offset:96\n\tds_read_b64 v[138:139], v228 offset:104\n\t" \
    "ds_read_b128 v[140:143], v229 offset:112\n\ts_waitcnt lgkmcnt(9)\n\tv_mfma_f32_32x32x16_bf16 v[178:193], v[112:115], v[116:119], v[178:193]\n\ts_waitcnt lgkmcnt(6)\n\t" \
    "v_mfma_f32_32x32x16_bf16 v[178:193], v[120:123], v[124:127], v[178:193]\n\ts_waitcnt lgkmcnt(3)\n\tv_mfma_f32_32x32x16_bf16 v[178:193], v[128:131], v[132:135], v[178:193]\n\ts_waitcnt lgkmcnt(0)\n\t" \
    "v_mfma_f32_32x32x16_bf16 v[178:193], v[136:139], v[140:143], v[178:193]\n\ts_branch G%=\n\tF%=:\n\ts_waitcnt lgkmcnt(0)\n\t" \
    "ds_read_b64_tr_b16 v[112:113], v228 offset:48\n\tds_read_b64_tr_b16 v[114:115], v228 offset:304\n\tds_read_b64 v[116:117], v229 offset:0\n\tds_read_b64 v[118:119], v229 offset:8\n\t" \
    "ds_read_b64_tr_b16 v[120:121], v228 offset:1072\n\tds_read_b64_tr_b16 v[122:123], v228 offset:1328\n\tds_read_b64 v[124:125], v229 offset:32\n\tds_read_b64 v[126:127], v229 offset:40\n\t" \
    "ds_read_b64_tr_b16 v[128:129], v228 offset:2208\n\tds_read_b64_tr_b16 v[130:131], v228 offset:2464\n\tds_read_b64 v[132:133], v229 offset:768\n\tds_read_b64 v[134:135], v229 offset:776\n\t" \
    "ds_read_b64_tr_b16 v[136:137], v228 offset:3232\n\tds_read_b64_tr_b16 v[138:139], v228 offset:3488\n\tds_read_b64 v[140:141], v229 offset:800\n\tds_read_b64 v[142:143], v229 offset:808\n\t" \
    "s_waitcnt lgkmcnt(12)\n\tv_mfma_f32_32x32x16_bf16 v[178:193], v[112:115], v[116:119], v[178:193]\n\tds_read_b64_tr_b16 v[112:113], v228 offset:16\n\tds_read_b64_tr_b16 v[114:115], v228 offset:272\n\t" \
    "ds_read_b64 v[116:117], v229 offset:1536\n\tds_read_b64 v[118:119], v229 offset:1544\n\ts_waitcnt lgkmcnt(12)\n\tv_mfma_f32_32x32x16_bf16 v[178:193], v[120:123], v[124:127], v[178:193]\n\t" \
    "ds_read_b64_tr_b16 v[120:121], v228 offset:1040\n\tds_read_b64_tr_b16 v[122:123], v228 offset:1296\n\tds_read_b64 v[124:125], v229 offset:1568\n\tds_read_b64 v[126:127], v229 offset:1576\n\t" \
    "s_waitcnt lgkmcnt(12)\n\tv_mfma_f32_32x32x16_bf16 v[178:193], v[128:131], v[132:135], v[178:193]\n\tds_read_b64_tr_b16 v[128:129], v228 offset:2176\n\tds_read_b64_tr_b16 v[130:131], v228 offset:2432\n\t" \
    "ds_read_b64 v[132:133], v229 offset:2304\n\tds_read_b64 v[134:135], v229 offset:2312\n\ts_waitcnt lgkmcnt(12)\n\tv_mfma_f32_32x32x16_bf16 v[178:193], v[136:139], v[140:143], v[178:193]\n\t" \
    "ds_read_b64_tr_b16 v[136:137], v228 offset:3200\n\tds_read_b64_tr_b16 v[138:139], v228 offset:3456\n\tds_read_b64 v[140:141], v229 offset:2336\n\tds_read_b64 v[142:143], v229 offset:2344\n\t" \
    "s_waitcnt lgkmcnt(12)\n\tv_mfma_f32_32x32x16_bf16 v[178:193], v[112:115], v[116:119], v[178:193]\n\ts_waitcnt lgkmcnt(8)\n\tv_mfma_f32_32x32x16_bf16 v[178:193], v[120:123], v[124:127], v[178:193]\n\t" \
    "s_waitcnt lgkmcnt(4)\n\tv_mfma_f32_32x32x16_bf16 v[178:193], v[128:131], v[132:135], v[178:193]\n\ts_waitcnt lgkmcnt(0)\n\tv_mfma_f32_32x32x16_bf16 v[178:193], v[136:139], v[140:143], v[178:193]\n\t" \
    "G%=:\n\ts_nop 15\n\t"

static_assert(WS_END == 0xac0000 && 4 * KVT == 0x2400, "constants inside the loop text");

struct Params {
    const u16 *x, *wqkv, *wo, *are, *aim, *bre, *bim, *cre, *cim, *logdt, *dskip, *gw;
    u16* out; char* ws;
};
__device__ __forceinline__ void lds_set(unsigned a) { asm volatile("" ::: "memory"); *(volatile LAS unsigned*)a = 1u; }
__device__ __forceinline__ unsigned lds_add(unsigned a) { asm volatile("" ::: "memory"); return __hip_atomic_fetch_add((LAS unsigned*)a, 1u, __ATOMIC_RELAXED, __HIP_MEMORY_SCOPE_WORKGROUP); }
__device__ __forceinline__ void lds_wait(unsigned a, unsigned n) {
#pragma unroll 1
    for (int sp = 0; sp < (1 << 22) && *(volatile LAS unsigned*)a < n; ++sp) __builtin_amdgcn_s_sleep(1);
    asm volatile("" ::: "memory");
}
__global__ void __launch_bounds__(512, 2) fused_kernel(Params P) {
    char* const ws = P.ws;
    const int tid = threadIdx.x, lane = tid & 63, wid = __builtin_amdgcn_readfirstlane(tid >> 6), bid = blockIdx.x;
    const int xcd = bid & 7, jb = bid >> 3;
    const int hg = (xcd % (8 / NA)) * NA + (jb % NA), rt = (xcd / (8 / NA)) * (32 / NA) + jb / NA;
    const int ort = 4 * xcd + (jb >> 3), oc = jb & 7;
    const int c0 = 64 * hg + 2 * rt;
    const int r = lane & 31, hh = lane >> 5, g = lane >> 4, r16 = lane & 15;
    STAMP_DECL; STAMP(0);
    asm volatile("" :: "s"(P.x), "s"(P.wqkv), "s"(P.are), "s"(P.aim), "s"(P.bre), "s"(P.bim), "s"(P.cre), "s"(P.cim), "s"(P.logdt), "s"(P.dskip), "s"(P.gw), "s"(P.ws), "s"(P.wo), "s"(P.out));
    u64 pc; asm volatile("s_getpc_b64 %0" : "=s"(pc));
    const int tt = wid & 1, pp = (wid >> 1) & 1;
    f32x16 zero16;
#pragma unroll
    for (int i = 0; i < 16; ++i) zero16[i] = 0.f;
    const __amdgpu_buffer_rsrc_t rs = __builtin_amdgcn_make_buffer_rsrc(ws, 0, WS_END, 0x00020000);
    f32x32 O;
    f32x16 yk_, SB, NM;
    u32x8 Q;
    u32x8 VA;
    const int hd = wid & 3, kh = wid >> 2;
    const unsigned voffK = lane * 16, voffV = r < 16 ? 1024 + hh * 256 + r * 16 : (r == 16 || r == 20) ? 1536 : 0x4000000;
    auto init_state = [&]() {
        asm volatile("v_mov_b64 v[0:1], 0\n\tv_mov_b64 v[2:3], 0\n\tv_mov_b64 v[4:5], 0\n\tv_mov_b64 v[6:7], 0\n\tv_mov_b64 v[8:9], 0\n\tv_mov_b64 v[10:11], 0\n\tv_mov_b64 v[12:13], 0\n\tv_mov_b64 v[14:15], 0\n\tv_mov_b64 v[16:17], 0\n\tv_mov_b64 v[18:19], 0\n\tv_mov_b64 v[20:21], 0\n\tv_mov_b64 v[22:23], 0\n\tv_mov_b64 v[24:25], 0\n\tv_mov_b64 v[26:27], 0\n\tv_mov_b64 v[28:29], 0\n\tv_mov_b64 v[30:31], 0\n\tv_mov_b64 v[48:49], 0\n\tv_mov_b64 v[50:51], 0\n\tv_mov_b64 v[52:53], 0\n\tv_mov_b64 v[54:55], 0\n\tv_mov_b64 v[56:57], 0\n\tv_mov_b64 v[58:59], 0\n\tv_mov_b64 v[60:61], 0\n\tv_mov_b64 v[62:63], 0\n\t" : "={v[0:31]}"(O), "={v[48:63]}"(SB));
#pragma unroll
        for (int i = 0; i < 4; ++i) { VA[i] = voffK + i * KVT; VA[4 + i] = voffV + i * KVT; }
        asm volatile("" : "+{v[104:111]}"(VA)); };
    auto rdq = [&]() {
        lds_wait(L_CTL + C_Q, 4);
        const u32x4 qa = ldsr<u32x4>(L_QX + hd * 2048 + lane * 16), qb = ldsr<u32x4>(L_QX + hd * 2048 + 1024 + lane * 16);
        const f32x2 sh = ldsr<f32x2>(L_SH + hd * 512 + lane * 8);
        const float nm = -fmaxf(sh[0], sh[1]);
#pragma unroll
        for (int i = 0; i < 16; ++i) { NM[i] = nm; if (i < 4) { Q[i] = qa[i]; Q[4 + i] = qb[i]; } }
        asm volatile("" : "+{v[64:79]}"(NM), "+{v[96:103]}"(Q)); };
    u64 tag;
    unsigned dk2;

    if (__builtin_expect(wid < 4, 1)) {
        const int c = wid & 1;
        dk2 = ((CAS const unsigned*)P.dskip)[c0 >> 1];
        if (wid < 2) {
            unsigned vx[4], vw[4];
#pragma unroll
            for (int j = 0; j < 4; ++j) {
                vx[j] = (8 * wid + 16 * j + (lane >> 3)) * 1024 + (((lane & 7) ^ g ^ (4 * wid)) << 4);
                vw[j] = (8 * wid + 16 * j + (lane >> 3)) * 3072 + (((((lane & 7) >> 1) ^ ((g & 1) << 1)) * 2 + (lane & 1)) << 4);
            }
            const char* const xb = (const char*)P.x + rt * 65536;
            const char* const wb = (const char*)P.wqkv + hg * 128;
            auto dma4 = [&](const char* s, const unsigned* v, unsigned dst) {
#pragma unroll
                for (int j = 0; j < 4; ++j) glds_s(s, v[j], dst + wid * 1024 + j * 2048); };
            auto dmaX = [&](int cc) { dma4(xb + ((cc + hg) & 7) * 128, vx, slot_at(cc)); };
            auto dmaW = [&](int cc) { const int kc = (cc + hg) & 7; const unsigned sl = slot_at(cc);
                dma4(wb + kc * 196608 + 1024, vw, sl + 8192); dma4(wb + kc * 196608 + 2048, vw, sl + 16384); dma4(wb + kc * 196608, vw, sl + 24576); };
            auto dmaC = [&](int cc) { if (ORD == 2) { dmaW(cc); dmaX(cc); } else { dmaX(cc); dmaW(cc); } };
            __builtin_amdgcn_s_setprio(3);
            u64 tg;
            asm volatile("global_load_dwordx2 %0, %1, %2 sc1" : "=v"(tg) : "v"(WS_NONCE), "s"(ws) : "memory");
            if (ORD == 3) { dmaX(0); dmaX(1); dmaW(0); dmaW(1); }
            else { dmaC(0); dmaC(1); dmaC(2); }
            STAMP(14);
            VBAR(12); dmaC(2);
#pragma unroll
            for (int cc = 1; cc < 7; ++cc) { VBAR(16); if (cc < 6) dmaC(cc + 2); }
            asm volatile("" : "+v"(tg));
            tag = tg + 1;
            const u64 f0 = __hip_atomic_load((GAS u64*)(ws + WS_F0 + (hg * 32 + r) * 256), RLX_AGENT);
            VBAR(1);
            u32x4 uv[4];
            auto ldu = [&]() {
#pragma unroll
                for (int i = 0; i < 4; ++i) uv[i] = __builtin_amdgcn_raw_buffer_load_b128(rs, WS_XT + (hg * 32 + 8 * i + 4 * c + (lane >> 4)) * 8192 + ((lane >> 1) & 7) * 1024 + (2 * rt + (lane & 1)) * 16, 0, SC1);
                SBAR(); };
            const bool seen = __all(f0 == tag);
            if (seen) ldu();
            if (!SEARLY) asm volatile("s_barrier" ::: "memory");
            STAMP(2);
            if (!seen) { wait_flags((GAS u64*)(ws + WS_F0 + (hg * 32 + r) * 256), tag); ldu(); }
#pragma unroll
            for (int i = 0; i < 3; ++i) if (i < 2 || lane < 32) ldsw<u32x4>(L_UB + c * UBC + (lane + 64 * i) * 16, u32x4{0, 0, 0, 0});
#pragma unroll
            for (int i = 0; i < 4; ++i) ldsw<u32x4>(L_UB + (lane & 1) * UBC + (32 + 16 * i + 8 * c + (lane >> 3)) * 80 + ((lane >> 1) & 3) * 16, uv[i]);
            if (lane == 0) lds_add(L_CTL + C_KV);
            lds_wait(L_CTL + C_KV, 2);
            if (lane < 6) ldsw<u32x4>(L_WS + c * WSC + (lane / 3) * 2176 + (lane % 3) * 16, u32x4{0, 0, 0, 0});
            u32x2 up[2], un[2]; u32x4 uc[2];
#pragma unroll
            for (int ps = 0; ps < 2; ++ps) {
                auto ua = [&](int q) -> unsigned { return L_UB + c * UBC + (q >> 2) * 80 + (q & 3) * 16; };
                const int q = 127 + 64 * ps + lane;
                up[ps] = ldsr<u32x2>(ua(q - 1) + 8); uc[ps] = ldsr<u32x4>(ua(q)); un[ps] = ldsr<u32x2>(ua(q + 1));
            }
            SBAR();
#pragma unroll
            for (int ps = 0; ps < 2; ++ps) {
                float v[16], w7[8], w9[8];
#pragma unroll
                for (int i = 0; i < 8; ++i) { const unsigned d = i < 2 ? up[ps][i] : i < 6 ? uc[ps][i - 2] : un[ps][i - 6]; v[2 * i] = bf2f(d & 0xffff); v[2 * i + 1] = bfhi(d); }
                w7[0] = ((v[1] + v[2]) + (v[3] + v[4])) + ((v[5] + v[6]) + v[7]);
#pragma unroll
                for (int i = 0; i < 8; ++i) { if (i) w7[i] = w7[i - 1] + (v[i + 7] - v[i]); w9[i] = w7[i] + (v[i] + v[i + 8]); }
                const unsigned o = L_WS + c * WSC + (3 + 64 * ps + lane) * 16;
                ldsw<u32x4>(o, u32x4{pk2(w7[0], w7[1]), pk2(w7[2], w7[3]), pk2(w7[4], w7[5]), pk2(w7[6], w7[7])});
                ldsw<u32x4>(o + 2176, u32x4{pk2(w9[0], w9[1]), pk2(w9[2], w9[3]), pk2(w9[4], w9[5]), pk2(w9[6], w9[7])});
            }
            STAMP(16);
        } else {
            const int po = (c0 + c) * 64 + lane;
            const unsigned pr = ((const unsigned*)P.are)[po], pi = ((const unsigned*)P.aim)[po], br = ((const unsigned*)P.bre)[po], bi = ((const unsigned*)P.bim)[po],
                           cr = ((const unsigned*)P.cre)[po], ci = ((const unsigned*)P.cim)[po], ld2 = ((CAS const unsigned*)P.logdt)[c0 >> 1];
            tag = __hip_atomic_load((GAS u64*)(ws + WS_NONCE), RLX_AGENT) + 1;
            const unsigned w0 = *(const GAS unsigned*)(WV == 1 ? (const char*)P.wqkv + (bid * 48 + c * 24 + (lane < 24 ? lane : 23)) * 128 : WV == 2 ? (const char*)P.gw :
                                                       WV == 3 ? (const char*)P.wqkv + (jb * 16 + c * 8 + (lane & 7)) * 3072 + (lane < 24 ? lane >> 3 : 0) * 1024 + hg * 128 :
                                                       WV == 4 ? (const char*)P.wqkv + ((lane & 7) * 64 + (jb / NA) * 2 * NA + c * NA + (lane / 24) % NA) * 3072 + ((lane % 24) >> 3) * 1024 + hg * 128 :
                                                       c ? (const char*)P.x + (bid * 64 + lane) * 128 : (const char*)P.wqkv + (bid * 48 + (lane < 48 ? lane : 47)) * 128);
            const unsigned w1 = *(const GAS unsigned*)(c ? (const char*)(pc & ~127ull) + (jb * CODE_LINES + (lane < CODE_LINES ? lane : 0)) * 128 : (const char*)P.wo + (bid * 16 + (lane & 15)) * 128);
            SBAR();
            const float dt = rbf(__expf(c ? bfhi(ld2) : bf2f(ld2 & 0xffff)));
            const float l0 = dt * bf2f(pr & 0xffff) * 1.4426950408889634f, l1 = dt * bfhi(pr) * 1.4426950408889634f;
            const float v0 = -dt * bf2f(pi & 0xffff) * 0.15915494309189535f, v1 = -dt * bfhi(pi) * 0.15915494309189535f;
            const C2 gg = cexp2(l0, v0, l1, v1, 256.f), e4 = cexp2(l0, v0, l1, v1, 4.f), e1 = cexp2(l0, v0, l1, v1, 1.f), e8 = cmul2(e4, e4);
            C2 a = cmul2(C2{f32x2{bf2f(cr & 0xffff), -bf2f(ci & 0xffff)}, f32x2{bfhi(cr), -bfhi(ci)}}, C2{f32x2{dt * bf2f(br & 0xffff), -dt * bf2f(bi & 0xffff)}, f32x2{dt * bfhi(br), -dt * bfhi(bi)}});
            C2 a2 = cmul2(a, gg);
            u32x2 hv[27];
            hv[8] = cpack(a2);
#pragma unroll
            for (int j = 0; j < 9; ++j) {
                if (j < 8) hv[j] = cpack(a);
                if (j && !(j & 1)) hv[8 + (j >> 1)] = cpack(cmul2(a, gg));
                if (j < 8) a = cmul2(a, e1);
                if (j < 6) { const int k = j + 2, row = k < 4 ? 13 + 3 * (k - 2) : 19 + 2 * (k - 4); a2 = cmul2(a2, gg); hv[row] = cpack(a2);
                    if (k < 4) hv[row + 1] = cpack(cmul2(a2, e4)); hv[row + (k < 4 ? 2 : 1)] = cpack(cmul2(a2, e8)); }
            }
#pragma unroll
            for (int i = 0; i < 27; ++i) if (i != 21 && i != 23 && i != 25) asm volatile("" : "+v"(hv[i]));
            STAMP(23);
#define BARE() do { SBAR(); asm volatile("s_barrier" ::: "memory"); SBAR(); } while (0)
            const C2 e = cexp2(l0, -v0, l1, -v1, 8.f);
            a = C2{f32x2{1.f, 0.f}, f32x2{1.f, 0.f}}; a2 = cexp2(l0, -v0, l1, -v1, 128.f);
            u32x2 lv[32];
#define LO(j0, j1) _Pragma("unroll") for (int j = (j0); j < (j1); ++j) { lv[j] = cpack(a); lv[16 + j] = cpack(a2); asm volatile("" : "+v"(lv[j]), "+v"(lv[16 + j])); \
                if (j < 15) { a = cmul2s(a, e); a2 = cmul2s(a2, e); } }
            BARE();
            u32x2 t[8];
#pragma unroll
            for (int n = 0; n < 8; ++n) { const int row = 32 * ((n >> 1) & 1) + 8 * g + 4 * (n & 1) + (r16 >> 2);
                t[n] = trread(slot_at(0) + row * 128 + (((4 * c + 2 * (n >> 2) + ((lane & 3) >> 1)) ^ ((row >> 1) & 7)) << 4) + (lane & 1) * 8); }
            const unsigned o = WS_XT + (hg * 32 + rt) * 8192 + g * 1024 + (32 * c + r16) * 16;
#define XST(n) __builtin_amdgcn_raw_buffer_store_b128(u32x4{t[2 * (n)][0], t[2 * (n)][1], t[2 * (n) + 1][0], t[2 * (n) + 1][1]}, rs, o + ((n) & 1) * 4096 + ((n) >> 1) * 256, 0, SC1)
            LO(0, 2); XST(0); XST(1); asm volatile("s_waitcnt lgkmcnt(0)" ::: "memory");
            BARE(); XST(2); XST(3); LO(2, 4);
            BARE();
            __builtin_amdgcn_raw_buffer_store_b128(u32x4{0x3f803f80u, 0x3f803f80u, 0x3f803f80u, 0x3f803f80u}, rs,
                                                   WS_KV + ((4 * hg + g) * 64 + 2 * rt + c) * KVT + 1536 + ((lane >> 3) & 1) * 640 + (lane & 7) * 16, 0, SC1);
            LO(4, 6);
            BARE(); LO(6, 8);
            asm volatile("s_waitcnt vmcnt(0)" :: "v"(w0), "v"(w1) : "memory");
            BARE();
            if (wid == 2 && lane == 0) __hip_atomic_store((GAS u64*)(ws + WS_F0 + (hg * 32 + rt) * 256), tag, RLX_AGENT);
            LO(8, 10); BARE(); LO(10, 13); BARE(); LO(13, 16); BARE(); if (!SEARLY) BARE();
            __builtin_amdgcn_s_setprio(3);
            STAMP(2);
#pragma unroll
            for (int i = 0; i < 3; ++i) ldsw<u32x4>(L_DT + c * DTC + i * 1024 + lane * 16, u32x4{0, 0, 0, 0});
#pragma unroll
            for (int i = 0; i < 32; ++i) { ldsw<u32x2>(L_LO + c * LOC + i * TROW + lane * 8, lv[i]); if (i < 27 && i != 21 && i != 23 && i != 25) ldsw<u32x2>(L_HI + c * HIC + i * TROW + lane * 8, hv[i]); }
            asm volatile("" ::: "memory");
            const u64 mp = r < 8 ? 0x0706050403020100ull : r < 16 ? 0x121110100b0a0908ull : r < 24 ? 0x000000120f0e0d0cull : 0x1a00180016001413ull;
            const unsigned aa = L_HI + c * HIC + (unsigned)((mp >> ((r & 7) * 8)) & 0xff) * TROW + hh * 16, ba = L_LO + c * LOC + r * TROW + hh * 16;
            STAMP(16);
            bf16x8 ta[16], tb[16];
#pragma unroll
            for (int s = 0; s < 4; ++s) { ta[s] = ldsr<bf16x8>(aa + s * 32); tb[s] = ldsr<bf16x8>(ba + s * 32); }
            SBAR();
            f32x16 k = zero16;
#pragma unroll
            for (int s = 0; s < 16; ++s) { k = MFMA32(ta[s], tb[s], k); SBAR(); if (s < 12) { ta[s + 4] = ldsr<bf16x8>(aa + s * 32 + 128); tb[s + 4] = ldsr<bf16x8>(ba + s * 32 + 128); } SBAR(); }
            STAMP(15);
            asm volatile("" ::: "memory");
            const unsigned K0 = L_LO + c * LOC, R = K0 + (1080 - 8 * r) * 2;
            ldsw<u32x2>(R + (1 - hh) * 8, u32x2{pk2(k[3], k[2]), pk2(k[1], k[0])});
            ldsw<u32x4>(R - (32 + 64 * hh) * 16, u32x4{pk2(k[8], k[7]), pk2(k[6], k[6]), pk2(k[6], k[5]), pk2(k[4], k[4])});
            if (hh == 0) ldsw<u32x4>(R - 64 * 16, u32x4{pk2(k[11], k[11]), pk2(k[10], k[10]), pk2(k[10], k[9]), pk2(k[9], k[9])});
            if (lane < 16) {
                const unsigned a2_ = lane ? 0u : pk2(k[12], k[12]);
                ldsw<u32x4>(lane < 8 ? K0 + (7 - lane) * 16 : K0 + (128 + lane) * 16, u32x4{0u, a2_ & 0xffff0000u, a2_, a2_});
            }
#pragma unroll
            for (int e_ = 0; e_ < 2; ++e_) {
                const u16 hv_ = __builtin_bit_cast(u16, (__bf16)k[13 + 2 * e_]);
                const unsigned d = L_DT + c * DTC + (r & 3) * 768 + (31 - 8 * (2 * hh + e_) - (r >> 2)) * 2;
#pragma unroll
                for (int q = 0; q < 4; ++q) ldsw<u16>(d + q * 194, hv_);
            }
            lds_set(L_CTL + C_K0 + 4 * c);
            STAMP(17);
        }
        {
            init_state();
            if (wid < 2) lds_wait(L_CTL + C_K0 + 4 * c, 1);
            const unsigned K0 = L_LO + c * LOC + (wid >> 1) * 1152;
            u32x4 cu[2]; u32x2 pv[2];
#pragma unroll
            for (int ps = 0; ps < 2; ++ps) { const unsigned a_ = K0 + (ps ? 64 + (lane & 7) : lane) * 16; cu[ps] = ldsr<u32x4>(a_); pv[ps] = ldsr<u32x2>(a_ - 8); }
            SBAR();
#pragma unroll
            for (int ps = 0; ps < 2; ++ps) {
                const unsigned am = __builtin_amdgcn_alignbit(pv[ps][1], pv[ps][0], 16), a0 = __builtin_amdgcn_alignbit(cu[ps][0], pv[ps][1], 16), a1 = __builtin_amdgcn_alignbit(cu[ps][1], cu[ps][0], 16),
                               a2_ = __builtin_amdgcn_alignbit(cu[ps][2], cu[ps][1], 16), a3 = __builtin_amdgcn_alignbit(cu[ps][3], cu[ps][2], 16);
                const unsigned W = K0 + KCP + (ps ? 64 + (lane & 7) : lane) * 16;
                if (ps == 0 || lane < 8) { ldsw<u32x4>(W, u32x4{a0, a1, a2_, a3}); ldsw<u32x4>(W + KCP, u32x4{pv[ps][1], cu[ps][0], cu[ps][1], cu[ps][2]}); ldsw<u32x4>(W + 2 * KCP, u32x4{am, a0, a1, a2_}); }
            }
        }
        if (lane == 0) lds_add(L_CTL + C_U);

        STAMP(6);
        __builtin_amdgcn_s_setprio(CVP);
        f32x32 Y;
        asm volatile("v_mov_b64 v[162:163], 0\n\tv_mov_b64 v[164:165], 0\n\tv_mov_b64 v[166:167], 0\n\tv_mov_b64 v[168:169], 0\n\tv_mov_b64 v[170:171], 0\n\tv_mov_b64 v[172:173], 0\n\tv_mov_b64 v[174:175], 0\n\tv_mov_b64 v[176:177], 0\n\tv_mov_b64 v[178:179], 0\n\tv_mov_b64 v[180:181], 0\n\tv_mov_b64 v[182:183], 0\n\tv_mov_b64 v[184:185], 0\n\tv_mov_b64 v[186:187], 0\n\tv_mov_b64 v[188:189], 0\n\tv_mov_b64 v[190:191], 0\n\tv_mov_b64 v[192:193], 0\n\t" : "={v[162:193]}"(Y));
        lds_wait(L_CTL + C_U, 4);
        STAMP(31);
        const int e = wid >> 1, kap = (r + 1) & 3;
        const unsigned ab = L_LO + c * LOC + kap * KCP + (1087 - r + kap + 8 * hh) * 2, bb = L_UB + c * UBC + (32 + r) * 80 + hh * 16;
        unsigned ap = ab - 64 * (16 * e + 15), bp = bb - 80 * (16 * e + 15);
        const unsigned wp = e ? L_WS + c * WSC + (8 * hh + (r16 >> 2)) * 64 + (g & 1) * 32 + (lane & 3) * 8 : ab - 64 * 33;
        const unsigned dp = e ? L_DT + c * DTC + kap * 192 + (31 - r + kap + 8 * hh) * 2 : bb - 80;
        { unsigned n_ = 4;
          asm volatile(CONV_LOOP : "+{v[162:193]}"(Y), "+{v226}"(ap), "+{v227}"(bp), [n] "+s"(n_) : "{v228}"(wp), "{v229}"(dp), [e] "s"(e)
                       : "scc", "memory", "v112", "v113", "v114", "v115", "v116", "v117", "v118", "v119", "v120", "v121", "v122", "v123", "v124", "v125", "v126", "v127", "v128", "v129", "v130", "v131", "v132", "v133", "v134", "v135", "v136", "v137", "v138", "v139", "v140", "v141", "v142", "v143", "v144", "v145", "v146", "v147", "v148", "v149", "v150", "v151", "v152", "v153", "v154", "v155", "v156", "v157", "v158", "v159"); }
        const f32x16 y0 = __builtin_shufflevector(Y, Y, 0, 1, 2, 3, 4, 5, 6, 7, 8, 9, 10, 11, 12, 13, 14, 15), y1 = __builtin_shufflevector(Y, Y, 16, 17, 18, 19, 20, 21, 22, 23, 24, 25, 26, 27, 28, 29, 30, 31);
        __builtin_amdgcn_s_setprio(3);
        STAMP(27);
        GAS u64* const f1p = (GAS u64*)(ws + WS_F1 + (hg * 32 + r) * 256);
        const u64 f1 = __hip_atomic_load(f1p, RLX_AGENT);
        const unsigned pA = L_PA + wid * 4096;
        if (e) {
#pragma unroll
            for (int i = 0; i < 4; ++i) ldsw<f32x4>(pA + i * 1024 + lane * 16, f32x4{y0[4 * i], y0[4 * i + 1], y0[4 * i + 2], y0[4 * i + 3]});
            yk_ = y1;
        } else {
#pragma unroll
            for (int i = 0; i < 4; ++i) ldsw<f32x4>(pA + i * 1024 + lane * 16, f32x4{y1[4 * i], y1[4 * i + 1], y1[4 * i + 2], y1[4 * i + 3]});
            yk_ = y0;
        }
        asm volatile("s_waitcnt lgkmcnt(0)" ::: "memory");
        lds_set(L_CTL + C_E + 4 * wid);
        if (!__all(f1 == tag)) wait_flags(f1p, tag);
        if (wid == 0) lds_set(L_CTL + C_WQ);
        STAMP(7);
    } else {
        if (wid == 4 && lane < 32) ldsw<unsigned>(L_CTL + lane * 4, 0u);
        __builtin_amdgcn_s_setprio(2);
        tag = __hip_atomic_load((GAS u64*)(ws + WS_NONCE), RLX_AGENT) + 1;
        const int w4 = wid - 4, grow = rt * 64 + 8 * hg + 2 * w4;
        u32x4 gv, gx0, gx1;
        unsigned xa[2][4], wk[2];
#pragma unroll
        for (int ks = 0; ks < 4; ++ks) { xa[0][ks] = (32 * tt + r) * 128 + (((2 * ks + hh) ^ ((r >> 1) & 7)) << 4); xa[1][ks] = xa[0][ks] + 2 * SLOT; asm volatile("" : "+v"(xa[1][ks])); }
        wk[0] = (8 * hh + (r16 >> 2)) * 128 + (((2 * pp + (g & 1)) ^ (r16 >> 3 << 1)) << 5) + (lane & 3) * 8; wk[1] = wk[0] + 2 * SLOT;
        asm volatile("" : "+v"(wk[0]), "+v"(wk[1]));
        f32x16 ak = zero16, av = zero16, aq = zero16, sq[2];
        float gsum;
        u32x4 kown[2];
        bf16x8 fx[4];
        u32x2 fw[4][3][2];
#define SH(cc) (slot_at(cc) >= 2 * SLOT)
#define SO(cc) (slot_at(cc) % (2 * SLOT))
#define RX(cc, ks) fx[ks] = ldsr<bf16x8>(xa[SH(cc)][ks] + SO(cc))
#define RW(cc, ks, m) do { fw[ks][m][0] = trread(wk[SH(cc)] + SO(cc) + 8192 * ((m) + 1) + (ks) * 2048); fw[ks][m][1] = trread(wk[SH(cc)] + SO(cc) + 8192 * ((m) + 1) + (ks) * 2048 + 512); } while (0)
#define FR(ks, m) __builtin_bit_cast(bf16x8, u32x4{fw[ks][m][0][0], fw[ks][m][0][1], fw[ks][m][1][0], fw[ks][m][1][1]})
#define MK(ks) ak = MFMA32(FR(ks, 0), fx[ks], ak); SBAR()
#define MV(ks) av = MFMA32(fx[ks], FR(ks, 1), av); SBAR()
#define MQ(ks) aq = MFMA32(FR(ks, 2), fx[ks], aq); SBAR()
#pragma unroll
        for (int cc = 0; cc < 8; ++cc) {
            STAMP(14 + cc);
            BAR();
            if (cc == 5) {
                gv = *(const u32x4*)(P.gw + lane * 8); gx0 = *(const u32x4*)(P.x + grow * 512 + lane * 8); gx1 = *(const u32x4*)(P.x + grow * 512 + 512 + lane * 8);
                SBAR();
            }
            if (cc) { MK(2); } RX(cc, 0); RW(cc, 0, 0); SBAR();
            if (cc) { MV(2); } RW(cc, 0, 1); SBAR();
            if (cc) { MQ(2); } RW(cc, 0, 2); RX(cc, 1); SBAR();
            if (cc) { MK(3); } RW(cc, 1, 0); RW(cc, 1, 1); SBAR();
            if (cc) { MV(3); } RW(cc, 1, 2); RX(cc, 2); SBAR();
            if (cc) { MQ(3); } RW(cc, 2, 0); RW(cc, 2, 1); SBAR();
            MK(0); RW(cc, 2, 2); RX(cc, 3); SBAR();
            MV(0); RW(cc, 3, 0); RW(cc, 3, 1); SBAR();
            MQ(0); RW(cc, 3, 2); SBAR();
            MK(1); MV(1); MQ(1);
        }
        if (!SEARLY) BAR();
        MK(2); MV(2); MK(3); MV(3); MQ(2); MQ(3);
        {
            float s0 = 0.f, s1 = 0.f;
#pragma unroll
            for (int j = 0; j < 4; ++j) { const unsigned gj = gv[j], aj = gx0[j], bj = gx1[j];
                s0 = __builtin_amdgcn_fdot2_f32_bf16(__builtin_bit_cast(bf16x2, aj), __builtin_bit_cast(bf16x2, gj), s0, false);
                s1 = __builtin_amdgcn_fdot2_f32_bf16(__builtin_bit_cast(bf16x2, bj), __builtin_bit_cast(bf16x2, gj), s1, false);
            }
            const auto sa = __builtin_amdgcn_permlane32_swap(__float_as_uint(s0), __float_as_uint(s1), false, false);
            gsum = __uint_as_float(sa[0]) + __uint_as_float(sa[1]);
            asm volatile("" : "+v"(gsum)); SBAR();
        }
        STAMP(1);
        {
            const unsigned tb = WS_KV + ((4 * hg + 2 * pp) * 64 + 2 * rt + tt) * KVT;
            const int rp = (r & 19) | ((r & 4) << 1) | ((r & 8) >> 1);
            const unsigned vb = tb + (r >> 4) * (64 * KVT) + 1024;
#pragma unroll
            for (int e = 0; e < 2; ++e) {
                const u32x4 kp = {pk2(ak[8 * e], ak[8 * e + 1]), pk2(ak[8 * e + 2], ak[8 * e + 3]), pk2(ak[8 * e + 4], ak[8 * e + 5]), pk2(ak[8 * e + 6], ak[8 * e + 7])};
                kown[e] = kp;
                __builtin_amdgcn_raw_buffer_store_b128(kp, rs, tb + e * (64 * KVT) + hh * 512 + rp * 16, 0, SC1);
                __builtin_amdgcn_raw_buffer_store_b128(u32x4{pk2(av[8 * e], av[8 * e + 1]), pk2(av[8 * e + 2], av[8 * e + 3]), pk2(av[8 * e + 4], av[8 * e + 5]), pk2(av[8 * e + 6], av[8 * e + 7])},
                                                       rs, vb + e * 640 + hh * 256 + r16 * 16, 0, SC1);
            }
        }
        STAMP(2);
        __builtin_amdgcn_s_setprio(GPRIO);
        {
            const auto sc = __builtin_amdgcn_permlane16_swap(__float_as_uint(gsum), __float_as_uint(gsum), false, false);
            float t = __uint_as_float(sc[0]) + __uint_as_float(sc[1]);
            t += __uint_as_float(__builtin_amdgcn_update_dpp(0u, __float_as_uint(t), 0x128, 0xf, 0xf, false));
            t += __uint_as_float(__builtin_amdgcn_update_dpp(0u, __float_as_uint(t), 0x124, 0xf, 0xf, false));
            t += __uint_as_float(__builtin_amdgcn_update_dpp(0u, __float_as_uint(t), 0x122, 0xf, 0xf, false));
            t += __uint_as_float(__builtin_amdgcn_update_dpp(0u, __float_as_uint(t), 0x121, 0xf, 0xf, false));
            const float gate = rbf(__builtin_amdgcn_rcpf(1.0f + __expf(-rbf(t))));
            if (r == 0) __builtin_amdgcn_raw_buffer_store_b32(__float_as_uint(gate), rs, WS_G + (grow + hh) * 4, 0, SC1);
        }
        u32x4 qp[2]; float mx[2];
#pragma unroll
        for (int e_ = 0; e_ < 2; ++e_) {
            qp[e_] = u32x4{pk2(aq[8 * e_] * QSCALE, aq[8 * e_ + 1] * QSCALE), pk2(aq[8 * e_ + 2] * QSCALE, aq[8 * e_ + 3] * QSCALE),
                           pk2(aq[8 * e_ + 4] * QSCALE, aq[8 * e_ + 5] * QSCALE), pk2(aq[8 * e_ + 6] * QSCALE, aq[8 * e_ + 7] * QSCALE)};
            ldsw<u32x4>(L_QX + ((2 * pp + e_) * 2 + tt) * 1024 + lane * 16, qp[e_]);
            sq[e_] = MFMA32(__builtin_bit_cast(bf16x8, kown[e_]), __builtin_bit_cast(bf16x8, qp[e_]), zero16);
        }
#pragma unroll
        for (int e_ = 0; e_ < 2; ++e_) ldsw<float>(L_SH + (2 * pp + e_) * 512 + lane * 8 + tt * 4, rowmax(sq[e_]));
        if (lane == 0) lds_add(L_CTL + C_Q);
        STAMP(23);
        asm volatile("s_waitcnt vmcnt(0)" ::: "memory");
        if (lane == 0 && lds_add(L_CTL + C_R7) == 3u) __hip_atomic_store((GAS u64*)(ws + WS_F1 + (hg * 32 + rt) * 256), tag, RLX_AGENT);
        STAMP(3);
        init_state();
        rdq();
        STAMP(25);
        if (wid == 4) {
            wait_flags((GAS u64*)(ws + WS_F1 + (hg * 32 + (rt & 16) + ((2 * ROT + lane % 6) & 15)) * 256), tag);
            if (lane == 0) lds_add(L_CTL + C_F);
        }
    }
    u32x32 R0; u32x16 R1; f32x16 SA;
    STAMP(24);
    if (wid >= 4) lds_wait(L_CTL + C_F, 1);
    STAMP(4);
    const unsigned kvh = WS_KV + (4 * hg + hd) * 64 * KVT;
    const unsigned kvb = kvh + 32 * (kh ^ (rt >> 4) ^ 1) * KVT;
    unsigned soc = kvb + (ROT & 7) * 4 * KVT, kk = ROT + 1;
    const u32x4 rsw = {(unsigned)(uintptr_t)ws, (unsigned)((uintptr_t)ws >> 32) & 0xffffu, WS_END, 0x00020000u};
    u32x4 t3[9];
#pragma unroll
    for (int u = 0; u < 3; ++u) {
        t3[3 * u + 2] = __builtin_amdgcn_raw_buffer_load_b128(rs, voffK + u * KVT, soc, SC1);
        t3[3 * u] = __builtin_amdgcn_raw_buffer_load_b128(rs, voffV + u * KVT, soc, SC1);
        t3[3 * u + 1] = __builtin_amdgcn_raw_buffer_load_b128(rs, voffV + u * KVT + 640, soc, SC1);
    }
    SBAR();
    u32x4 bst[2];
    if (!kh) {
        const int c = wid & 1, e = wid >> 1;
        lds_wait(L_CTL + C_E + 4 * (wid ^ 2), 1);
        const float dsk = c ? bfhi(dk2) : bf2f(dk2 & 0xffff);
        f32x4 p[4]; u32x2 u[4];
#pragma unroll
        for (int i = 0; i < 4; ++i) { p[i] = ldsr<f32x4>(L_PA + (wid ^ 2) * 4096 + i * 1024 + lane * 16); u[i] = ldsr<u32x2>(L_UB + c * UBC + (32 + 32 * e + r) * 80 + i * 16 + hh * 8); }
        SBAR();
#pragma unroll
        for (int i = 0; i < 4; ++i)
            ldsw<u32x2>(L_PA + 16384 + wid * 2048 + r * 64 + i * 16 + hh * 8,
                        u32x2{pk2(fmaf(bf2f(u[i][0] & 0xffff), dsk, yk_[4 * i] + p[i][0]), fmaf(bfhi(u[i][0]), dsk, yk_[4 * i + 1] + p[i][1])),
                              pk2(fmaf(bf2f(u[i][1] & 0xffff), dsk, yk_[4 * i + 2] + p[i][2]), fmaf(bfhi(u[i][1]), dsk, yk_[4 * i + 3] + p[i][3]))});
        asm volatile("" ::: "memory");
#pragma unroll
        for (int i = 0; i < 2; ++i) bst[i] = ldsr<u32x4>(L_PA + 16384 + wid * 2048 + i * 1024 + lane * 16);
        rdq();
        asm volatile("s_waitcnt lgkmcnt(0)" ::: "memory");
        if (lane == 0) lds_add(L_CTL + C_TAB);
    }
    {
        SA = MFMA32(__builtin_bit_cast(bf16x8, t3[2]), __builtin_bit_cast(bf16x8, u32x4{Q[0], Q[1], Q[2], Q[3]}), NM);
#pragma unroll
        for (int i = 0; i < 32; ++i) R0[i] = t3[i >> 2][i & 3];
#pragma unroll
        for (int i = 0; i < 16; ++i) R1[i] = i < 4 ? t3[8][i] : 0u;
    }
    STAMP(5);
#define ATTN_IO "+{v[0:31]}"(O), "+{v[32:47]}"(SA), "+{v[48:63]}"(SB), "+{v[64:79]}"(NM), "+{v[96:103]}"(Q), "+{v[104:111]}"(VA), "+{v[112:143]}"(R0), "+{v[144:159]}"(R1), \
                [soc] "+s"(soc), [k] "+s"(kk), [n] "+s"(n_), [son] "=&s"(son_)
#define ATTN_IN "{v160}"(SMF_IDX), [rs] "s"(rsw), [base] "s"(kvb), [lim] "s"(ROT + 8), [yk] "s"(yk)
#define ATTN(trips) do { unsigned n_ = (trips), son_; asm volatile(ATTN_PLAIN : ATTN_IO : ATTN_IN : "scc", "memory"); } while (0)
    const unsigned yk = kh ? ROT + YTRIP + 2 : ~0u;
    asm volatile("" : "+{v[112:143]}"(R0), "+{v[144:159]}"(R1));
    if (!kh) {
#pragma unroll
        for (int i = 0; i < 2; ++i) __builtin_amdgcn_raw_buffer_store_b128(bst[i], rs, WS_BT + (c0 + (wid & 1)) * 4096 + (wid >> 1) * 2048 + i * 1024 + lane * 16, 0, SC1);
    }
    __builtin_amdgcn_s_setprio(1);
    ATTN(2);
    if (!kh) {
        asm volatile("s_waitcnt vmcnt(0)" ::: "memory");
        if (lane == 0 && lds_add(L_CTL + C_CONV) == 3u) __hip_atomic_store((GAS u64*)(ws + WS_F2 + (hg * 32 + rt) * 256), tag, RLX_AGENT);
        lds_wait(L_CTL + C_TAB, 4);
        const unsigned vo = (128 * hd + (lane >> 3)) * 1024 + (((((lane & 7) >> 1) ^ g) * 2 + (lane & 1)) << 4);
        const char* s = (const char*)P.wo + oc * 128;
#pragma unroll
        for (int i = 0; i < 16; ++i) glds_s(s + i * 8192, vo, L_WO + hd * 16384 + i * 1024);
    } else { lds_wait(L_CTL + C_WQ, 1); lds_wait(L_CTL + C_TAB, 4); __builtin_amdgcn_s_setprio(2); }
    STAMP(26);
    ATTN(6);
    asm volatile("s_waitcnt vmcnt(0)" : "+{v[112:143]}"(R0), "+{v[144:159]}"(R1) :: "memory");
    f32x16 oa, ob;
#pragma unroll
    for (int i = 0; i < 16; ++i) { oa[i] = O[i]; ob[i] = O[16 + i]; }
    ob = SMF(__builtin_bit_cast(bf16x8, u32x4{R1[8], R1[9], R1[10], R1[11]}), hi8(SB), ob);
    STAMP(8);
    {
        const unsigned xw = L_XB + wid * XBW, xr = L_XB + (wid ^ 4) * XBW;
        const f32x16 os = kh ? oa : ob; f32x16 o = kh ? ob : oa;
        ldsw<f32x4>(xw + lane * 16, f32x4{os[0], os[1], os[2], os[3]}); ldsw<f32x4>(xw + 1024 + lane * 16, f32x4{os[4], os[5], os[6], os[7]});
        ldsw<float>(xw + 2048 + lane * 4, os[8]);
        __builtin_amdgcn_s_setprio(1);
        lds_set(L_CTL + C_M + 4 * wid);
        STAMP(30);
        lds_wait(L_CTL + C_M + 4 * (wid ^ 4), 1);
        const float pl = ldsr<float>(xr + 2048 + lane * 4);
        const f32x4 p0 = ldsr<f32x4>(xr + lane * 16), p1 = ldsr<f32x4>(xr + 1024 + lane * 16);
        SBAR();
        float l = o[8] + pl;
        const bool safe = l < 0x1p60f && l > 0x1p-30f;
#pragma unroll
        for (int i = 0; i < 4; ++i) { o[i] += p0[i]; o[4 + i] += p1[i]; }
        if (!__all(safe)) {
            const bf16x8 q = __builtin_bit_cast(bf16x8, kh ? u32x4{Q[4], Q[5], Q[6], Q[7]} : u32x4{Q[0], Q[1], Q[2], Q[3]}); float m = -INFINITY; o = zero16;
            for (int t = 0; t < 64; ++t) {
                const unsigned so = kvh + t * KVT;
                const bf16x8 k = __builtin_bit_cast(bf16x8, __builtin_amdgcn_raw_buffer_load_b128(rs, voffK, so, SC1));
                const bf16x8 v0 = __builtin_bit_cast(bf16x8, __builtin_amdgcn_raw_buffer_load_b128(rs, voffV, so, SC1));
                const bf16x8 v1 = __builtin_bit_cast(bf16x8, __builtin_amdgcn_raw_buffer_load_b128(rs, voffV + 640, so, SC1));
                f32x16 s = MFMA32(k, q, zero16);
                const float m2 = fmaxf(m, rowmax(s)), al = EXP2(m - m2); m = m2;
#pragma unroll
                for (int i = 0; i < 16; ++i) { s[i] = EXP2(s[i] - m); o[i] *= al; }
                o = SMF(v0, lo8(s), o); o = SMF(v1, hi8(s), o);
            }
            l = o[8];
        }
        const float rl = __builtin_amdgcn_rcpf(l);
        __builtin_amdgcn_raw_buffer_store_b128(u32x4{pk2(o[0] * rl, o[1] * rl), pk2(o[2] * rl, o[3] * rl), pk2(o[4] * rl, o[5] * rl), pk2(o[6] * rl, o[7] * rl)}, rs,
                                               WS_OF + ((rt * 4 + 2 * kh + (r >> 4)) * 16 + 2 * hg + (hd >> 1)) * 1024 + (2 * (hd & 1) + hh) * 256 + r16 * 16, 0, SC1);
    }
    asm volatile("s_waitcnt vmcnt(0)" ::: "memory");
    if (lane == 0 && lds_add(L_CTL + C_O) == 7u) __hip_atomic_store((GAS u64*)(ws + WS_F3 + (rt * 8 + hg) * 256), tag, RLX_AGENT);
    lds_wait(L_CTL + C_O, 8);
    STAMP(9);
    const int rg = wid & 3;
    bf16x8 wf[8][4];
#pragma unroll
    for (int m = 0; m < 4; ++m) {
        const unsigned a = L_WO + (256 * kh + 16 * (g >> 1) + 4 * (g & 1) + (r16 >> 2)) * 128 + ((m ^ (2 * (g & 1) + (r16 >> 3))) << 5) + (lane & 3) * 8;
#pragma unroll
        for (int ks = 0; ks < 8; ++ks) wf[ks][m] = trfrag(a + ks * 4096, a + ks * 4096 + 1024);
    }
    if (wid == 0) {
        wait_flags((GAS u64*)(ws + (lane < 32 ? WS_F2 + (oc * 32 + lane) * 256 : WS_F3 + (ort * 8 + (lane & 7)) * 256)), tag);
        if (bid == 0 && lane == 0) __hip_atomic_store((GAS u64*)(ws + WS_NONCE), tag, RLX_AGENT);
    }
    BAR();
    STAMP(10);
    {
        bf16x8 of[8];
#pragma unroll
        for (int ks = 0; ks < 8; ++ks) of[ks] = __builtin_bit_cast(bf16x8, __builtin_amdgcn_raw_buffer_load_b128(rs, WS_OF + ((ort * 4 + rg) * 16 + 8 * kh + ks) * 1024 + lane * 16, 0, SC1));
        const u32x4 bv = __builtin_amdgcn_raw_buffer_load_b128(rs, WS_BT + (64 * oc + (tid >> 3)) * 4096 + ort * 128 + (tid & 7) * 16, 0, SC1);
        const float gate = __uint_as_float(__builtin_amdgcn_raw_buffer_load_b32(rs, WS_G + (ort * 64 + 16 * rg + r16) * 4, 0, SC1));
        SBAR();

        f32x4 acc[4];
#pragma unroll
        for (int m = 0; m < 4; ++m) acc[m] = f32x4{0.f, 0.f, 0.f, 0.f};
#pragma unroll
        for (int ks = 0; ks < 8; ++ks)
#pragma unroll
            for (int m = 0; m < 4; ++m) acc[m] = MFMA16(wf[ks][m], of[ks], acc[m]);
        STAMP(29);
        ldsw<u32x4>(L_BI + (tid >> 3) * BROW + (tid & 7) * 16, bv);
        const f32x4 k0 = kh ? acc[2] : acc[0], k1 = kh ? acc[3] : acc[1], s0 = kh ? acc[0] : acc[2], s1 = kh ? acc[1] : acc[3];
        ldsw<f32x4>(L_RED + wid * 2048 + lane * 16, s0); ldsw<f32x4>(L_RED + wid * 2048 + 1024 + lane * 16, s1);
        BAR();
        STAMP(11);
        const float omg = rbf(1.0f - gate);
        u32x2 w[2];
#pragma unroll
        for (int e = 0; e < 2; ++e) {
            const f32x4 a = (e ? k1 : k0) + ldsr<f32x4>(L_RED + (wid ^ 4) * 2048 + e * 1024 + lane * 16);
            const u32x2 b = trread(L_BI + (16 * (2 * kh + e) + 4 * g + (r16 >> 2)) * BROW + rg * 32 + (lane & 3) * 8);
            w[e] = u32x2{pk2(fmaf(gate, a[0], omg * bf2f(b[0] & 0xffff)), fmaf(gate, a[1], omg * bfhi(b[0]))), pk2(fmaf(gate, a[2], omg * bf2f(b[1] & 0xffff)), fmaf(gate, a[3], omg * bfhi(b[1])))};
        }
        const auto z0 = __builtin_amdgcn_permlane16_swap(w[0][0], w[1][0], false, false), z1 = __builtin_amdgcn_permlane16_swap(w[0][1], w[1][1], false, false);
        const unsigned ob_ = L_XB + wid * 1280;
        ldsw<u32x4>(ob_ + r16 * 80 + (2 * (g & 1) + (g >> 1)) * 16, u32x4{z0[0], z1[0], z0[1], z1[1]});
        asm volatile("" ::: "memory");
        __builtin_amdgcn_raw_buffer_store_b128(ldsr<u32x4>(ob_ + (lane >> 2) * 80 + (lane & 3) * 16), __builtin_amdgcn_make_buffer_rsrc(P.out, 0, 2097152, 0x00020000),
                                               ((ort * 64 + 16 * rg + (lane >> 2)) * 512 + 64 * oc + 32 * kh) * 2 + (lane & 3) * 16, 0, SC1);
    }
    STAMP(12); STAMP_OUT();
}

extern "C" void kernel_launch(void* const* d_in, const int* in_sizes, int n_in, void* d_out, int out_size, void* d_ws, size_t ws_size, hipStream_t stream) {
    Params p;
    p.x = (const u16*)d_in[0]; p.wqkv = (const u16*)d_in[1]; p.wo = (const u16*)d_in[2];
    p.are = (const u16*)d_in[4]; p.aim = (const u16*)d_in[5]; p.bre = (const u16*)d_in[6]; p.bim = (const u16*)d_in[7];
    p.cre = (const u16*)d_in[8]; p.cim = (const u16*)d_in[9]; p.logdt = (const u16*)d_in[10]; p.dskip = (const u16*)d_in[11]; p.gw = (const u16*)d_in[12];
    p.out = (u16*)d_out; p.ws = (char*)d_ws;
    (void)hipFuncSetAttribute((const void*)fused_kernel, hipFuncAttributeMaxDynamicSharedMemorySize, LDS_BYTES);
    fused_kernel<<<256, 512, LDS_BYTES, stream>>>(p);
}
```
